```python
import torch

def _softmax(x, axis=-1):
    return torch.softmax(x, dim=axis)
def _gelu(x):
    return torch.nn.functional.gelu(x, approximate="tanh")
def _silu(x):
    return torch.nn.functional.silu(x)
def _sigmoid(x):
    return torch.sigmoid(x)
def _layernorm(x, axis=-1, eps=1e-5):
    m = x.mean(dim=axis, keepdim=True)
    v = x.var(dim=axis, keepdim=True, unbiased=False)
    return (x - m) / torch.sqrt(v + eps)
def _scatter_add(tgt, idx, src, axis=0):
    return tgt.clone().index_add_(axis, idx, src)
def _segment_sum(src, seg, n):
    out = torch.zeros((n,) + src.shape[1:], dtype=src.dtype)
    return out.index_add_(0, seg, src)
def _pad(x, pw):
    flat = tuple(v for pair in reversed(pw) for v in pair)
    return torch.nn.functional.pad(x, flat)

kernel_name = 'recurrent_scan__sliding_block_cached__swiglu_ffn__decode'

def setup_inputs():

    _w = (lambda a: (lambda t: t.to(torch.bfloat16) if t.is_floating_point() else t)(torch.as_tensor(a)))
    def _A_setup():
        B, Sq, D = 1, 1, 576
        Hq, Hkv, Sk, Dh = 32, 16, 16384, 128
        assert Hq % Hkv == 0, (Hq, Hkv)
        return {
            "x": _w((lambda *s: torch.randn(*s, dtype=torch.bfloat16) * 0.05)(B, Sq, D)),
            "k_cache": _w((lambda *s: torch.randn(*s, dtype=torch.bfloat16) * 1.6)(B, Hkv, Sk, Dh)),
            "v_cache": _w((lambda *s: torch.randn(*s, dtype=torch.bfloat16) * 0.2)(B, Hkv, Sk, Dh)),
            "wq": _w((lambda *s: torch.randn(*s, dtype=torch.bfloat16) * 1.6)(D, Hq * Dh) * (D ** -0.5)),
            "wo": _w((lambda *s: torch.randn(*s, dtype=torch.bfloat16) * 0.2)(Hq * Dh, D) * ((Hq * Dh) ** -0.5)),
        }
    _A_in = _A_setup()

    def _B_setup():
        B, S, D, F = 1, 1, 576, 4608
        return {
            "x": _w((lambda *s: torch.randn(*s, dtype=torch.bfloat16) * 0.05)(B, S, D)),
            "w_gate": _w((lambda *s: torch.randn(*s, dtype=torch.bfloat16) * 0.1)(D, F)),
            "w_up": _w((lambda *s: torch.randn(*s, dtype=torch.bfloat16) * 0.1)(D, F)),
            "w_down": _w((lambda *s: torch.randn(*s, dtype=torch.bfloat16) * 0.1)(F, D)),
        }
    _B_in = _B_setup()

    return {
        "A_x": _A_in["x"],
        "A_k_cache": _A_in["k_cache"],
        "A_v_cache": _A_in["v_cache"],
        "A_wq": _A_in["wq"],
        "A_wo": _A_in["wo"],
        "B_x": _B_in["x"],
        "B_w_gate": _B_in["w_gate"],
        "B_w_up": _B_in["w_up"],
        "B_w_down": _B_in["w_down"],
    }

def reference(A_x, A_k_cache, A_v_cache, A_wq, A_wo, B_x, B_w_gate, B_w_up, B_w_down):
    def _A_ref(x, k_cache, v_cache, wq, wo):
        B, Sq, D = x.shape
        Hq, Dh = 32, 128
        Hkv, Sk = k_cache.shape[1], k_cache.shape[2]
        W = min(4096, max(256, Sk // 4))
        rep = Hq // Hkv
        q = (lambda x, *d: x.permute(*d))((x @ wq).reshape(B, Sq, Hq, Dh), 0, 2, 1, 3)
        k = (lambda x, n, axis: torch.repeat_interleave(x, n, dim=axis))(k_cache[:, :, Sk - W:, :], rep, axis=1)
        v = (lambda x, n, axis: torch.repeat_interleave(x, n, dim=axis))(v_cache[:, :, Sk - W:, :], rep, axis=1)
        scores = torch.einsum("bhqd,bhkd->bhqk", q, k) * (Dh ** -0.5)
        p = _softmax(scores, axis=-1)
        o = torch.einsum("bhqk,bhkd->bhqd", p, v)
        return (lambda x, *d: x.permute(*d))(o, 0, 2, 1, 3).reshape(B, Sq, Hq * Dh) @ wo

    def _B_ref(x, w_gate, w_up, w_down):
        g = _silu(x @ w_gate)
        u = x @ w_up
        return (g * u) @ w_down

    x = A_x
    xn = _layernorm(x, axis=-1)
    s = _A_ref(xn, A_k_cache, A_v_cache, A_wq, A_wo)
    g = _silu(_B_ref(xn, B_w_gate, B_w_up, B_w_down))
    return x + s * g
```

```cpp
#include <hip/hip_runtime.h>
#include <hip/hip_bf16.h>
#include <cstdint>

typedef __hip_bfloat16 bf16;
using u32x4 = __attribute__((ext_vector_type(4))) unsigned;
using f32x4 = __attribute__((ext_vector_type(4))) float;
using bf16x2 = __attribute__((ext_vector_type(2))) __bf16;

constexpr int DM = 576;
constexpr int HQ = 32, HKV = 16, SK = 16384, DH = 128, WIN = 4096;
constexpr int NQ = HQ * DH;
constexpr int FF = 4608;
constexpr int NSPLIT = 16;
constexpr int KPS = WIN / NSPLIT;
constexpr int NTH = 576;
constexpr int NDOWN = 64;
constexpr int NWO = 64;

constexpr size_t WS_Q = 0;
constexpr size_t WS_H = 16384;
constexpr size_t WS_PML = 65536;
constexpr size_t WS_PO = WS_PML + 8192;
constexpr size_t WS_FP = WS_PO + 32 * 16 * 128 * 4;
constexpr size_t WS_SP = WS_FP + (size_t)NDOWN * DM * 4;

__device__ __forceinline__ float bf2f(unsigned short u) { return __uint_as_float(((unsigned)u) << 16); }
__device__ __forceinline__ float bf16r(float x) {
  unsigned u = __float_as_uint(x);
  u = (u + 0x7FFFu + ((u >> 16) & 1u)) & 0xFFFF0000u;
  return __uint_as_float(u);
}
__device__ __forceinline__ float lo_f(unsigned v) { return __uint_as_float(v << 16); }
__device__ __forceinline__ float hi_f(unsigned v) { return __uint_as_float(v & 0xFFFF0000u); }
__device__ __forceinline__ float dot2(unsigned k, unsigned q, float acc) {
  return __builtin_amdgcn_fdot2_f32_bf16(__builtin_bit_cast(bf16x2, k), __builtin_bit_cast(bf16x2, q), acc, false);
}
template <int CTRL> __device__ __forceinline__ float dpp(float x) {
  return __builtin_bit_cast(float, __builtin_amdgcn_mov_dpp(__builtin_bit_cast(int, x), CTRL, 0xf, 0xf, true));
}
constexpr int XOR1 = 0xB1, XOR2 = 0x4E, XOR7 = 0x141, XOR8 = 0x128, ROR4 = 0x124;
__device__ __forceinline__ float xrow16_max(float x) {
  auto s = __builtin_amdgcn_permlane16_swap(__float_as_uint(x), __float_as_uint(x), false, false);
  x = fmaxf(__uint_as_float(s[0]), __uint_as_float(s[1]));
  auto t = __builtin_amdgcn_permlane32_swap(__float_as_uint(x), __float_as_uint(x), false, false);
  return fmaxf(__uint_as_float(t[0]), __uint_as_float(t[1]));
}
__device__ __forceinline__ float xrow16_sum(float x) {
  auto s = __builtin_amdgcn_permlane16_swap(__float_as_uint(x), __float_as_uint(x), false, false);
  x = __uint_as_float(s[0]) + __uint_as_float(s[1]);
  auto t = __builtin_amdgcn_permlane32_swap(__float_as_uint(x), __float_as_uint(x), false, false);
  return __uint_as_float(t[0]) + __uint_as_float(t[1]);
}
__device__ __forceinline__ float wave_sum(float x) {
  x += dpp<XOR1>(x); x += dpp<XOR2>(x); x += dpp<XOR7>(x); x += dpp<XOR8>(x);
  return xrow16_sum(x);
}
__device__ __forceinline__ u32x4 ldnt(const void* p) { return __builtin_nontemporal_load(reinterpret_cast<const u32x4*>(p)); }

__device__ __forceinline__ void layernorm_576(const bf16* x, float* xn, float* red) {
  const int tid = threadIdx.x, lane = tid & 63, wave = tid >> 6;
  const float xv = bf2f(reinterpret_cast<const unsigned short*>(x)[tid]);
  float s = wave_sum(xv);
  if (lane == 0) red[wave] = s;
  __syncthreads();
  float tot = 0.f;
#pragma unroll
  for (int w = 0; w < 9; ++w) tot += red[w];
  const float mean = tot * (1.0f / 576.0f);
  const float dv = xv - mean;
  float s2 = wave_sum(dv * dv);
  if (lane == 0) red[16 + wave] = s2;
  __syncthreads();
  float tot2 = 0.f;
#pragma unroll
  for (int w = 0; w < 9; ++w) tot2 += red[16 + w];
  const float var = tot2 * (1.0f / 576.0f);
  const float m_bf = bf16r(mean), v_bf = bf16r(var);
  const float den = bf16r(sqrtf(bf16r(v_bf + 1e-5f)));
  xn[tid] = bf16r(bf16r(xv - m_bf) / den);
  __syncthreads();
}

__global__ void __launch_bounds__(NTH) k1_qh(const bf16* __restrict__ x, const bf16* __restrict__ wq,
                                             const bf16* __restrict__ wg, const bf16* __restrict__ wu,
                                             unsigned char* __restrict__ ws) {
  __shared__ float xn[DM];
  __shared__ float red[32];
  __shared__ float part[2][9][64];
  const int tid = threadIdx.x, lane = tid & 63, wave = tid >> 6;
  layernorm_576(x, xn, red);
  const bool isq = blockIdx.x < 64;
  const int cb = isq ? blockIdx.x : blockIdx.x - 64;
  const int N = isq ? NQ : FF;
  const int rr = lane >> 3, ch = lane & 7;
  const bf16* W0 = isq ? wq : wg;
  const int nmat = isq ? 1 : 2;
  for (int mt = 0; mt < nmat; ++mt) {
    const bf16* W = mt == 0 ? W0 : wu;
    u32x4 wv[8];
#pragma unroll
    for (int j = 0; j < 8; ++j) {
      const int row = wave * 64 + j * 8 + rr;
      wv[j] = ldnt(W + (size_t)row * N + cb * 64 + ch * 8);
    }
    float acc[8];
#pragma unroll
    for (int k = 0; k < 8; ++k) acc[k] = 0.f;
#pragma unroll
    for (int j = 0; j < 8; ++j) {
      const float xv = xn[wave * 64 + j * 8 + rr];
#pragma unroll
      for (int k = 0; k < 4; ++k) {
        acc[2 * k] = fmaf(xv, lo_f(wv[j][k]), acc[2 * k]);
        acc[2 * k + 1] = fmaf(xv, hi_f(wv[j][k]), acc[2 * k + 1]);
      }
    }
#pragma unroll
    for (int k = 0; k < 8; ++k) {
      float a = acc[k];
      a += dpp<XOR8>(a);
      a = xrow16_sum(a);
      acc[k] = a;
    }
    if (lane < 8) {
#pragma unroll
      for (int k = 0; k < 8; ++k) part[mt][wave][ch * 8 + k] = acc[k];
    }
  }
  __syncthreads();
  if (tid < 64) {
    float a = 0.f, b = 0.f;
#pragma unroll
    for (int w = 0; w < 9; ++w) { a += part[0][w][tid]; b += part[1][w][tid]; }
    if (isq) {
      const float qv = bf16r(a);
      reinterpret_cast<unsigned short*>(ws + WS_Q)[cb * 64 + tid] = (unsigned short)(__float_as_uint(qv) >> 16);
    } else {
      const float g = bf16r(a), u = bf16r(b);
      const float sg = bf16r(g / (1.0f + __expf(-g)));
      reinterpret_cast<float*>(ws + WS_H)[cb * 64 + tid] = bf16r(sg * u);
    }
  }
}

__global__ void __launch_bounds__(NTH) k2_attn(const bf16* __restrict__ kc, const bf16* __restrict__ vc,
                                               const bf16* __restrict__ wd, unsigned char* __restrict__ ws) {
  __shared__ __attribute__((aligned(16))) float lds[8 * 64 + 8 * 2 * (4 + DH) + 8 * DM];
  const int tid = threadIdx.x, lane = tid & 63, wave = tid >> 6;
  if (blockIdx.x >= HKV * NSPLIT) {
    const int b = blockIdx.x - HKV * NSPLIT;
    const int cg = tid % 72, rp = tid / 72;
    const float* h = reinterpret_cast<const float*>(ws + WS_H);
    u32x4 wv[9];
#pragma unroll
    for (int j = 0; j < 9; ++j) wv[j] = ldnt(wd + (size_t)(b * 72 + j * 8 + rp) * DM + cg * 8);
    float acc[8];
#pragma unroll
    for (int k = 0; k < 8; ++k) acc[k] = 0.f;
#pragma unroll
    for (int j = 0; j < 9; ++j) {
      const float hv = h[b * 72 + j * 8 + rp];
#pragma unroll
      for (int k = 0; k < 4; ++k) {
        acc[2 * k] = fmaf(hv, lo_f(wv[j][k]), acc[2 * k]);
        acc[2 * k + 1] = fmaf(hv, hi_f(wv[j][k]), acc[2 * k + 1]);
      }
    }
    float* red = lds;
    *reinterpret_cast<f32x4*>(red + rp * DM + cg * 8) = f32x4{acc[0], acc[1], acc[2], acc[3]};
    *reinterpret_cast<f32x4*>(red + rp * DM + cg * 8 + 4) = f32x4{acc[4], acc[5], acc[6], acc[7]};
    __syncthreads();
    float s = 0.f;
#pragma unroll
    for (int r = 0; r < 8; ++r) s += red[r * DM + tid];
    reinterpret_cast<float*>(ws + WS_FP)[(size_t)b * DM + tid] = s;
    return;
  }
  const int g = blockIdx.x / NSPLIT, sp = blockIdx.x % NSPLIT;
  const int r = lane >> 4, c = lane & 15;
  float* pl = lds;
  float* mrg = lds + 8 * 64;
  if (wave < 8) {
    const size_t key0 = (size_t)g * SK + (SK - WIN) + sp * KPS + wave * 32;
    const bf16* kb = kc + key0 * DH + r * DH + c * 8;
    const bf16* vb = vc + key0 * DH + r * DH + c * 8;
    u32x4 K[8], V[8];
#pragma unroll
    for (int i = 0; i < 8; ++i) K[i] = ldnt(kb + i * 4 * DH);
#pragma unroll
    for (int i = 0; i < 8; ++i) V[i] = ldnt(vb + i * 4 * DH);
    u32x4 q[2];
    const bf16* qp = reinterpret_cast<const bf16*>(ws + WS_Q) + (size_t)g * 2 * DH + c * 8;
    q[0] = *reinterpret_cast<const u32x4*>(qp);
    q[1] = *reinterpret_cast<const u32x4*>(qp + DH);
    float x[16];
#pragma unroll
    for (int i = 0; i < 8; ++i)
#pragma unroll
      for (int h = 0; h < 2; ++h) {
        float d = 0.f;
#pragma unroll
        for (int k = 0; k < 4; ++k) d = dot2(K[i][k], q[h][k], d);
        x[i * 2 + h] = d;
      }
#define TR_STEP(HALF, CTRL, BIT) _Pragma("unroll") for (int h = 0; h < (HALF); ++h) { \
      const float lo_ = x[h], hi_ = x[h + (HALF)]; const float t1_ = lo_ + dpp<CTRL>(lo_), t2_ = hi_ + dpp<CTRL>(hi_); \
      x[h] = (c & (BIT)) ? t2_ : t1_; }
    TR_STEP(8, XOR8, 8) TR_STEP(4, XOR7, 4) TR_STEP(2, XOR2, 2) TR_STEP(1, XOR1, 1)
#undef TR_STEP
    const float sc = bf16r(bf16r(x[0]) * 0.08838834764831845f);
    float pm = sc;
    pm = fmaxf(pm, dpp<XOR2>(pm));
    pm = fmaxf(pm, dpp<ROR4>(pm));
    pm = fmaxf(pm, dpp<XOR8>(pm));
    pm = xrow16_max(pm);
    const float p = __expf(sc - pm);
    float l = p;
    l += dpp<XOR2>(l); l += dpp<ROR4>(l); l += dpp<XOR8>(l); l = xrow16_sum(l);
    float* prow = pl + (wave * 4 + r) * 16;
    prow[c] = p;
    asm volatile("s_waitcnt lgkmcnt(0)" ::: "memory");
    f32x4 pr[4];
#pragma unroll
    for (int u = 0; u < 4; ++u) pr[u] = *reinterpret_cast<const f32x4*>(prow + 4 * u);
    float acc[16];
#pragma unroll
    for (int k = 0; k < 16; ++k) acc[k] = 0.f;
#pragma unroll
    for (int i = 0; i < 8; ++i) {
      float vf[8];
#pragma unroll
      for (int k = 0; k < 4; ++k) { vf[2 * k] = lo_f(V[i][k]); vf[2 * k + 1] = hi_f(V[i][k]); }
#pragma unroll
      for (int h = 0; h < 2; ++h) {
        const float ph = pr[(i * 2 + h) >> 2][(i * 2 + h) & 3];
#pragma unroll
        for (int k = 0; k < 8; ++k) acc[h * 8 + k] = fmaf(ph, vf[k], acc[h * 8 + k]);
      }
    }
#pragma unroll
    for (int k = 0; k < 16; ++k) acc[k] = xrow16_sum(acc[k]);
    if (r == 0) {
#pragma unroll
      for (int h = 0; h < 2; ++h) {
        float* rec = mrg + (wave * 2 + h) * (4 + DH);
        *reinterpret_cast<f32x4*>(rec + 4 + 8 * c) = f32x4{acc[h * 8 + 0], acc[h * 8 + 1], acc[h * 8 + 2], acc[h * 8 + 3]};
        *reinterpret_cast<f32x4*>(rec + 4 + 8 * c + 4) = f32x4{acc[h * 8 + 4], acc[h * 8 + 5], acc[h * 8 + 6], acc[h * 8 + 7]};
        if (c == h) { rec[0] = pm; rec[1] = l; }
      }
    }
  }
  __syncthreads();
  if (tid < 256) {
    const int h = tid >> 7, d = tid & 127;
    float M = -INFINITY;
#pragma unroll
    for (int w = 0; w < 8; ++w) M = fmaxf(M, mrg[(w * 2 + h) * (4 + DH)]);
    float L = 0.f, o = 0.f;
#pragma unroll
    for (int w = 0; w < 8; ++w) {
      const float* rec = mrg + (w * 2 + h) * (4 + DH);
      const float wt = __expf(rec[0] - M);
      L = fmaf(wt, rec[1], L);
      o = fmaf(wt, rec[4 + d], o);
    }
    const int head = g * 2 + h;
    reinterpret_cast<float*>(ws + WS_PO)[((size_t)head * NSPLIT + sp) * DH + d] = o;
    if (d == 0) {
      float* pml = reinterpret_cast<float*>(ws + WS_PML) + ((size_t)head * NSPLIT + sp) * 2;
      pml[0] = M; pml[1] = L;
    }
  }
}

__global__ void __launch_bounds__(NTH) k3_wo(const bf16* __restrict__ wo, unsigned char* __restrict__ ws) {
  __shared__ __attribute__((aligned(16))) float lds[64 + 8 * DM];
  const int tid = threadIdx.x;
  const int b = blockIdx.x;
  const int cg = tid % 72, rp = tid / 72;
  u32x4 wv[8];
#pragma unroll
  for (int j = 0; j < 8; ++j) wv[j] = ldnt(wo + (size_t)(b * 64 + j * 8 + rp) * DM + cg * 8);
  float* ov = lds;
  if (tid < 64) {
    const int head = b >> 1, d = (b & 1) * 64 + tid;
    const float* pml = reinterpret_cast<const float*>(ws + WS_PML) + (size_t)head * NSPLIT * 2;
    const float* po = reinterpret_cast<const float*>(ws + WS_PO) + (size_t)head * NSPLIT * DH + d;
    float M = -INFINITY;
#pragma unroll
    for (int s = 0; s < NSPLIT; ++s) M = fmaxf(M, pml[2 * s]);
    float L = 0.f, o = 0.f;
#pragma unroll
    for (int s = 0; s < NSPLIT; ++s) {
      const float wt = __expf(pml[2 * s] - M);
      L = fmaf(wt, pml[2 * s + 1], L);
      o = fmaf(wt, po[s * DH], o);
    }
    ov[tid] = bf16r(o / L);
  }
  __syncthreads();
  float acc[8];
#pragma unroll
  for (int k = 0; k < 8; ++k) acc[k] = 0.f;
#pragma unroll
  for (int j = 0; j < 8; ++j) {
    const float hv = ov[j * 8 + rp];
#pragma unroll
    for (int k = 0; k < 4; ++k) {
      acc[2 * k] = fmaf(hv, lo_f(wv[j][k]), acc[2 * k]);
      acc[2 * k + 1] = fmaf(hv, hi_f(wv[j][k]), acc[2 * k + 1]);
    }
  }
  float* red = lds + 64;
  *reinterpret_cast<f32x4*>(red + rp * DM + cg * 8) = f32x4{acc[0], acc[1], acc[2], acc[3]};
  *reinterpret_cast<f32x4*>(red + rp * DM + cg * 8 + 4) = f32x4{acc[4], acc[5], acc[6], acc[7]};
  __syncthreads();
  float s = 0.f;
#pragma unroll
  for (int r = 0; r < 8; ++r) s += red[r * DM + tid];
  reinterpret_cast<float*>(ws + WS_SP)[(size_t)b * DM + tid] = s;
}

__global__ void __launch_bounds__(NTH) k4_final(const bf16* __restrict__ x, const unsigned char* __restrict__ ws,
                                                bf16* __restrict__ out) {
  __shared__ float red[2][9][64];
  const int tid = threadIdx.x;
  const int col = blockIdx.x * 64 + (tid & 63), gr = tid >> 6;
  const float* sp = reinterpret_cast<const float*>(ws + WS_SP);
  const float* fp = reinterpret_cast<const float*>(ws + WS_FP);
  float s = 0.f, f = 0.f;
  for (int i = gr; i < NWO; i += 9) s += sp[(size_t)i * DM + col];
  for (int i = gr; i < NDOWN; i += 9) f += fp[(size_t)i * DM + col];
  red[0][gr][tid & 63] = s;
  red[1][gr][tid & 63] = f;
  __syncthreads();
  if (tid < 64) {
    float st = 0.f, ft = 0.f;
#pragma unroll
    for (int w = 0; w < 9; ++w) { st += red[0][w][tid]; ft += red[1][w][tid]; }
    const float sv = bf16r(st);
    const float fv = bf16r(ft);
    const float gv = bf16r(fv / (1.0f + __expf(-fv)));
    const float xv = bf2f(reinterpret_cast<const unsigned short*>(x)[col]);
    const float o = bf16r(xv + bf16r(sv * gv));
    reinterpret_cast<unsigned short*>(out)[col] = (unsigned short)(__float_as_uint(o) >> 16);
  }
}

extern "C" void kernel_launch(void* const* d_in, const int* in_sizes, int n_in,
                              void* d_out, int out_size, void* d_ws, size_t ws_size,
                              hipStream_t stream) {
  const bf16* x = (const bf16*)d_in[0];
  const bf16* kc = (const bf16*)d_in[1];
  const bf16* vc = (const bf16*)d_in[2];
  const bf16* wq = (const bf16*)d_in[3];
  const bf16* wo = (const bf16*)d_in[4];
  const bf16* wg = (const bf16*)d_in[6];
  const bf16* wu = (const bf16*)d_in[7];
  const bf16* wd = (const bf16*)d_in[8];
  unsigned char* ws = (unsigned char*)d_ws;
  k1_qh<<<64 + 72, NTH, 0, stream>>>(x, wq, wg, wu, ws);
  k2_attn<<<HKV * NSPLIT + NDOWN, NTH, 0, stream>>>(kc, vc, wd, ws);
  k3_wo<<<NWO, NTH, 0, stream>>>(wo, ws);
  k4_final<<<9, NTH, 0, stream>>>(x, ws, (bf16*)d_out);
}
```

```cpp
#include <hip/hip_runtime.h>
#include <hip/hip_bf16.h>
#include <cstdint>

typedef __hip_bfloat16 bf16;
using u32x4 = __attribute__((ext_vector_type(4))) unsigned;
using f32x4 = __attribute__((ext_vector_type(4))) float;
using bf16x2 = __attribute__((ext_vector_type(2))) __bf16;
typedef unsigned long long u64;

constexpr int DM = 576;
constexpr int HKV = 16, SK = 16384, DH = 128, WIN = 4096;
constexpr int NQ = 4096;
constexpr int FF = 4608;
constexpr int NSPLIT = 16;
constexpr int KPS = WIN / NSPLIT;
constexpr int NTH = 576;
constexpr int NWG = 256;
constexpr int NRED = 36;

constexpr int G_Q = 0;
constexpr int G_H = G_Q + NQ;
constexpr int G_PART = G_H + FF;
constexpr int G_SP = G_PART + 32 * NSPLIT * 130;
constexpr int G_END = G_SP + 128 * DM;
constexpr size_t GRAN_BYTES = (size_t)G_END * 8;
static_assert(GRAN_BYTES % 16 == 0, "memset size multiple of 16");

struct Params {
  const bf16* x; const bf16* kc; const bf16* vc; const bf16* wq; const bf16* wo;
  const bf16* wg; const bf16* wu; const bf16* wd; bf16* out; u64* gr;
};

__device__ __forceinline__ float bf2f(unsigned short u) { return __uint_as_float(((unsigned)u) << 16); }
__device__ __forceinline__ float bf16r(float x) {
  unsigned u = __float_as_uint(x);
  u = (u + 0x7FFFu + ((u >> 16) & 1u)) & 0xFFFF0000u;
  return __uint_as_float(u);
}
__device__ __forceinline__ float lo_f(unsigned v) { return __uint_as_float(v << 16); }
__device__ __forceinline__ float hi_f(unsigned v) { return __uint_as_float(v & 0xFFFF0000u); }
__device__ __forceinline__ float dot2(unsigned k, unsigned q, float acc) {
  return __builtin_amdgcn_fdot2_f32_bf16(__builtin_bit_cast(bf16x2, k), __builtin_bit_cast(bf16x2, q), acc, false);
}
template <int CTRL> __device__ __forceinline__ float dpp(float x) {
  return __builtin_bit_cast(float, __builtin_amdgcn_mov_dpp(__builtin_bit_cast(int, x), CTRL, 0xf, 0xf, true));
}
constexpr int XOR1 = 0xB1, XOR2 = 0x4E, XOR7 = 0x141, XOR8 = 0x128, ROR4 = 0x124;
__device__ __forceinline__ float xrow16_max(float x) {
  auto s = __builtin_amdgcn_permlane16_swap(__float_as_uint(x), __float_as_uint(x), false, false);
  x = fmaxf(__uint_as_float(s[0]), __uint_as_float(s[1]));
  auto t = __builtin_amdgcn_permlane32_swap(__float_as_uint(x), __float_as_uint(x), false, false);
  return fmaxf(__uint_as_float(t[0]), __uint_as_float(t[1]));
}
__device__ __forceinline__ float xrow16_sum(float x) {
  auto s = __builtin_amdgcn_permlane16_swap(__float_as_uint(x), __float_as_uint(x), false, false);
  x = __uint_as_float(s[0]) + __uint_as_float(s[1]);
  auto t = __builtin_amdgcn_permlane32_swap(__float_as_uint(x), __float_as_uint(x), false, false);
  return __uint_as_float(t[0]) + __uint_as_float(t[1]);
}
__device__ __forceinline__ float wave_sum(float x) {
  x += dpp<XOR1>(x); x += dpp<XOR2>(x); x += dpp<XOR7>(x); x += dpp<XOR8>(x);
  return xrow16_sum(x);
}
__device__ __forceinline__ u32x4 ldnt(const void* p) { return __builtin_nontemporal_load(reinterpret_cast<const u32x4*>(p)); }

__device__ __forceinline__ void st_gran(u64* g, unsigned v) {
  __hip_atomic_store(g, (1ull << 32) | (u64)v, __ATOMIC_RELAXED, __HIP_MEMORY_SCOPE_AGENT);
}
__device__ __forceinline__ u64 ld_gran(const u64* g) {
  return __hip_atomic_load(g, __ATOMIC_RELAXED, __HIP_MEMORY_SCOPE_AGENT);
}
constexpr int SPIN_CAP = 1 << 22;

__global__ void __launch_bounds__(NTH, 3) fused_kernel(Params p) {
  __shared__ __attribute__((aligned(16))) float s_xn[DM];
  __shared__ __attribute__((aligned(16))) float s_red[32];
  __shared__ __attribute__((aligned(16))) float s_part[2][9][64];
  __shared__ __attribute__((aligned(16))) unsigned short s_q[256];
  __shared__ __attribute__((aligned(16))) float s_p[8 * 64];
  __shared__ __attribute__((aligned(16))) float s_mrg[8 * 2 * (4 + DH)];
  __shared__ __attribute__((aligned(16))) float s_coef[72];
  __shared__ __attribute__((aligned(16))) float s_g2[8 * DM];
  __shared__ __attribute__((aligned(16))) float s_fin[2][NRED][16];

  const int tid = threadIdx.x, lane = tid & 63, wave = __builtin_amdgcn_readfirstlane(tid >> 6);
  const int b = blockIdx.x;
  u64* gr = p.gr;

  const float xv = bf2f(reinterpret_cast<const unsigned short*>(p.x)[tid]);
  const bool isq = b < 64, isgu = (b >= 64) && (b < 136);
  const int cb = isq ? b : b - 64;
  const int rr = lane >> 3, ch = lane & 7;
  u32x4 w1a[8], w1b[8];
  if (isq) {
#pragma unroll
    for (int j = 0; j < 8; ++j) w1a[j] = ldnt(p.wq + (size_t)(wave * 64 + j * 8 + rr) * NQ + cb * 64 + ch * 8);
  } else if (isgu) {
#pragma unroll
    for (int j = 0; j < 8; ++j) w1a[j] = ldnt(p.wg + (size_t)(wave * 64 + j * 8 + rr) * FF + cb * 64 + ch * 8);
#pragma unroll
    for (int j = 0; j < 8; ++j) w1b[j] = ldnt(p.wu + (size_t)(wave * 64 + j * 8 + rr) * FF + cb * 64 + ch * 8);
  }
  const int g = b >> 4, sp = b & 15;
  const int r = lane >> 4, c = lane & 15;
  u32x4 K[8], V[8];
  if (wave < 8) {
    const size_t key0 = (size_t)g * SK + (SK - WIN) + sp * KPS + wave * 32;
    const bf16* kb = p.kc + key0 * DH + r * DH + c * 8;
    const bf16* vb = p.vc + key0 * DH + r * DH + c * 8;
#pragma unroll
    for (int i = 0; i < 8; ++i) K[i] = ldnt(kb + i * 4 * DH);
#pragma unroll
    for (int i = 0; i < 8; ++i) V[i] = ldnt(vb + i * 4 * DH);
  }

  {
    float s = wave_sum(xv);
    if (lane == 0) s_red[wave] = s;
    __syncthreads();
    float tot = 0.f;
#pragma unroll
    for (int w = 0; w < 9; ++w) tot += s_red[w];
    const float mean = tot * (1.0f / 576.0f);
    const float dv = xv - mean;
    float s2 = wave_sum(dv * dv);
    if (lane == 0) s_red[16 + wave] = s2;
    __syncthreads();
    float tot2 = 0.f;
#pragma unroll
    for (int w = 0; w < 9; ++w) tot2 += s_red[16 + w];
    const float var = tot2 * (1.0f / 576.0f);
    const float m_bf = bf16r(mean), v_bf = bf16r(var);
    const float den = bf16r(sqrtf(bf16r(v_bf + 1e-5f)));
    s_xn[tid] = bf16r(bf16r(xv - m_bf) / den);
    __syncthreads();
  }

  if (isq || isgu) {
    float acc[8], acb[8];
#pragma unroll
    for (int k = 0; k < 8; ++k) { acc[k] = 0.f; acb[k] = 0.f; }
#pragma unroll
    for (int j = 0; j < 8; ++j) {
      const float xr = s_xn[wave * 64 + j * 8 + rr];
#pragma unroll
      for (int k = 0; k < 4; ++k) {
        acc[2 * k] = fmaf(xr, lo_f(w1a[j][k]), acc[2 * k]);
        acc[2 * k + 1] = fmaf(xr, hi_f(w1a[j][k]), acc[2 * k + 1]);
      }
    }
    if (isgu) {
#pragma unroll
      for (int j = 0; j < 8; ++j) {
        const float xr = s_xn[wave * 64 + j * 8 + rr];
#pragma unroll
        for (int k = 0; k < 4; ++k) {
          acb[2 * k] = fmaf(xr, lo_f(w1b[j][k]), acb[2 * k]);
          acb[2 * k + 1] = fmaf(xr, hi_f(w1b[j][k]), acb[2 * k + 1]);
        }
      }
    }
#pragma unroll
    for (int k = 0; k < 8; ++k) {
      float a = acc[k]; a += dpp<XOR8>(a); acc[k] = xrow16_sum(a);
      float bb = acb[k]; bb += dpp<XOR8>(bb); acb[k] = xrow16_sum(bb);
    }
    if (lane < 8) {
#pragma unroll
      for (int k = 0; k < 8; ++k) { s_part[0][wave][ch * 8 + k] = acc[k]; s_part[1][wave][ch * 8 + k] = acb[k]; }
    }
  }
  __syncthreads();
  if (tid < 64) {
    if (isq) {
      float a = 0.f;
#pragma unroll
      for (int w = 0; w < 9; ++w) a += s_part[0][w][tid];
      st_gran(gr + G_Q + cb * 64 + tid, __float_as_uint(bf16r(a)) >> 16);
    } else if (isgu) {
      float a = 0.f, bb = 0.f;
#pragma unroll
      for (int w = 0; w < 9; ++w) { a += s_part[0][w][tid]; bb += s_part[1][w][tid]; }
      const float gg = bf16r(a), uu = bf16r(bb);
      const float sg = bf16r(gg / (1.0f + __expf(-gg)));
      st_gran(gr + G_H + cb * 64 + tid, __float_as_uint(bf16r(sg * uu)));
    }
  }

  if (wave == 8) {
    const u64* qg = gr + G_Q + g * 256 + lane;
    unsigned qv[4];
    for (int spins = 0; spins < SPIN_CAP; ++spins) {
      bool ok = true;
#pragma unroll
      for (int k = 0; k < 4; ++k) { const u64 x = ld_gran(qg + k * 64); qv[k] = (unsigned)x; ok &= (x >> 32) == 1ull; }
      if (__all(ok)) break;
      __builtin_amdgcn_s_sleep(1);
    }
#pragma unroll
    for (int k = 0; k < 4; ++k) s_q[lane + k * 64] = (unsigned short)qv[k];
  }
  __syncthreads();
  u32x4 w2[9];
  if (wave < 8) {
    u32x4 q[2];
    q[0] = *reinterpret_cast<const u32x4*>(s_q + c * 8);
    q[1] = *reinterpret_cast<const u32x4*>(s_q + DH + c * 8);
    float x[16];
#pragma unroll
    for (int i = 0; i < 8; ++i)
#pragma unroll
      for (int h = 0; h < 2; ++h) {
        float d = 0.f;
#pragma unroll
        for (int k = 0; k < 4; ++k) d = dot2(K[i][k], q[h][k], d);
        x[i * 2 + h] = d;
      }
#define TR_STEP(HALF, CTRL, BIT) _Pragma("unroll") for (int h = 0; h < (HALF); ++h) { \
      const float lo_ = x[h], hi_ = x[h + (HALF)]; const float t1_ = lo_ + dpp<CTRL>(lo_), t2_ = hi_ + dpp<CTRL>(hi_); \
      x[h] = (c & (BIT)) ? t2_ : t1_; }
    TR_STEP(8, XOR8, 8) TR_STEP(4, XOR7, 4) TR_STEP(2, XOR2, 2) TR_STEP(1, XOR1, 1)
#undef TR_STEP
    const float sc = bf16r(bf16r(x[0]) * 0.08838834764831845f);
    float pm = sc;
    pm = fmaxf(pm, dpp<XOR2>(pm));
    pm = fmaxf(pm, dpp<ROR4>(pm));
    pm = fmaxf(pm, dpp<XOR8>(pm));
    pm = xrow16_max(pm);
    const float pr_ = __expf(sc - pm);
    float l = pr_;
    l += dpp<XOR2>(l); l += dpp<ROR4>(l); l += dpp<XOR8>(l); l = xrow16_sum(l);
    float* prow = s_p + (wave * 4 + r) * 16;
    prow[c] = pr_;
    asm volatile("s_waitcnt lgkmcnt(0)" ::: "memory");
    f32x4 pr[4];
#pragma unroll
    for (int u = 0; u < 4; ++u) pr[u] = *reinterpret_cast<const f32x4*>(prow + 4 * u);
    float acc[16];
#pragma unroll
    for (int k = 0; k < 16; ++k) acc[k] = 0.f;
#pragma unroll
    for (int i = 0; i < 8; ++i) {
      float vf[8];
#pragma unroll
      for (int k = 0; k < 4; ++k) { vf[2 * k] = lo_f(V[i][k]); vf[2 * k + 1] = hi_f(V[i][k]); }
#pragma unroll
      for (int h = 0; h < 2; ++h) {
        const float ph = pr[(i * 2 + h) >> 2][(i * 2 + h) & 3];
#pragma unroll
        for (int k = 0; k < 8; ++k) acc[h * 8 + k] = fmaf(ph, vf[k], acc[h * 8 + k]);
      }
    }
#pragma unroll
    for (int k = 0; k < 16; ++k) acc[k] = xrow16_sum(acc[k]);
    if (r == 0) {
#pragma unroll
      for (int h = 0; h < 2; ++h) {
        float* rec = s_mrg + (wave * 2 + h) * (4 + DH);
        *reinterpret_cast<f32x4*>(rec + 4 + 8 * c) = f32x4{acc[h * 8 + 0], acc[h * 8 + 1], acc[h * 8 + 2], acc[h * 8 + 3]};
        *reinterpret_cast<f32x4*>(rec + 4 + 8 * c + 4) = f32x4{acc[h * 8 + 4], acc[h * 8 + 5], acc[h * 8 + 6], acc[h * 8 + 7]};
        if (c == h) { rec[0] = pm; rec[1] = l; }
      }
    }
  }
  const int cg = tid % 72, rp = tid / 72;
  if (b < 64) {
#pragma unroll
    for (int j = 0; j < 8; ++j) w2[j] = ldnt(p.wo + (size_t)(b * 64 + j * 8 + rp) * DM + cg * 8);
  } else if (b < 128) {
#pragma unroll
    for (int j = 0; j < 9; ++j) w2[j] = ldnt(p.wd + (size_t)((b - 64) * 72 + j * 8 + rp) * DM + cg * 8);
  }
  __syncthreads();
  if (tid < 256) {
    const int h = tid >> 7, d = tid & 127;
    float M = -INFINITY;
#pragma unroll
    for (int w = 0; w < 8; ++w) M = fmaxf(M, s_mrg[(w * 2 + h) * (4 + DH)]);
    float L = 0.f, o = 0.f;
#pragma unroll
    for (int w = 0; w < 8; ++w) {
      const float* rec = s_mrg + (w * 2 + h) * (4 + DH);
      const float wt = __expf(rec[0] - M);
      L = fmaf(wt, rec[1], L);
      o = fmaf(wt, rec[4 + d], o);
    }
    u64* pg = gr + G_PART + ((g * 2 + h) * NSPLIT + sp) * 130;
    st_gran(pg + 2 + d, __float_as_uint(o));
    if (d == 0) { st_gran(pg, __float_as_uint(M)); st_gran(pg + 1, __float_as_uint(L)); }
  }

  if (b < 128) {
    if (wave == 8) {
      if (b < 64) {
        const int head = b >> 1, d = (b & 1) * 64 + lane;
        const u64* pg = gr + G_PART + head * NSPLIT * 130;
        unsigned mv[NSPLIT], lv[NSPLIT], ov[NSPLIT];
        for (int spins = 0; spins < SPIN_CAP; ++spins) {
          bool ok = true;
#pragma unroll
          for (int s = 0; s < NSPLIT; ++s) {
            const u64 a = ld_gran(pg + s * 130), bq = ld_gran(pg + s * 130 + 1), cq = ld_gran(pg + s * 130 + 2 + d);
            mv[s] = (unsigned)a; lv[s] = (unsigned)bq; ov[s] = (unsigned)cq;
            ok &= ((a >> 32) == 1ull) & ((bq >> 32) == 1ull) & ((cq >> 32) == 1ull);
          }
          if (__all(ok)) break;
          __builtin_amdgcn_s_sleep(1);
        }
        float M = -INFINITY;
#pragma unroll
        for (int s = 0; s < NSPLIT; ++s) M = fmaxf(M, __uint_as_float(mv[s]));
        float L = 0.f, o = 0.f;
#pragma unroll
        for (int s = 0; s < NSPLIT; ++s) {
          const float wt = __expf(__uint_as_float(mv[s]) - M);
          L = fmaf(wt, __uint_as_float(lv[s]), L);
          o = fmaf(wt, __uint_as_float(ov[s]), o);
        }
        s_coef[lane] = bf16r(o / L);
      } else {
        const u64* hg = gr + G_H + (b - 64) * 72;
        unsigned hv[2];
        for (int spins = 0; spins < SPIN_CAP; ++spins) {
          const u64 a = ld_gran(hg + lane);
          const u64 bq = ld_gran(hg + 64 + (lane & 7));
          hv[0] = (unsigned)a; hv[1] = (unsigned)bq;
          const bool ok = ((a >> 32) == 1ull) & ((bq >> 32) == 1ull);
          if (__all(ok)) break;
          __builtin_amdgcn_s_sleep(1);
        }
        s_coef[lane] = __uint_as_float(hv[0]);
        if (lane < 8) s_coef[64 + lane] = __uint_as_float(hv[1]);
      }
    }
    __syncthreads();
    float acc[8];
#pragma unroll
    for (int k = 0; k < 8; ++k) acc[k] = 0.f;
#pragma unroll
    for (int j = 0; j < 9; ++j) {
      if (j < 8 || b >= 64) {
        const float hv = s_coef[j * 8 + rp];
#pragma unroll
        for (int k = 0; k < 4; ++k) {
          acc[2 * k] = fmaf(hv, lo_f(w2[j][k]), acc[2 * k]);
          acc[2 * k + 1] = fmaf(hv, hi_f(w2[j][k]), acc[2 * k + 1]);
        }
      }
    }
    *reinterpret_cast<f32x4*>(s_g2 + rp * DM + cg * 8) = f32x4{acc[0], acc[1], acc[2], acc[3]};
    *reinterpret_cast<f32x4*>(s_g2 + rp * DM + cg * 8 + 4) = f32x4{acc[4], acc[5], acc[6], acc[7]};
    __syncthreads();
    float s = 0.f;
#pragma unroll
    for (int rq = 0; rq < 8; ++rq) s += s_g2[rq * DM + tid];
    st_gran(gr + G_SP + b * DM + tid, __float_as_uint(s));
  }

  if (b < NRED) {
    const int col = b * 16 + (tid & 15), grp = tid >> 4;
    const u64* sg = gr + G_SP + col;
    unsigned v[4];
    const bool a1 = (grp + 36) < 64;
    for (int spins = 0; spins < SPIN_CAP; ++spins) {
      bool ok = true;
      const u64 x0 = ld_gran(sg + (size_t)grp * DM);
      const u64 x1 = ld_gran(sg + (size_t)(a1 ? grp + 36 : grp) * DM);
      const u64 x2 = ld_gran(sg + (size_t)(64 + grp) * DM);
      const u64 x3 = ld_gran(sg + (size_t)(64 + (a1 ? grp + 36 : grp)) * DM);
      v[0] = (unsigned)x0; v[1] = (unsigned)x1; v[2] = (unsigned)x2; v[3] = (unsigned)x3;
      ok = ((x0 >> 32) == 1ull) & ((x1 >> 32) == 1ull) & ((x2 >> 32) == 1ull) & ((x3 >> 32) == 1ull);
      if (__all(ok)) break;
      __builtin_amdgcn_s_sleep(1);
    }
    s_fin[0][grp][tid & 15] = __uint_as_float(v[0]) + (a1 ? __uint_as_float(v[1]) : 0.f);
    s_fin[1][grp][tid & 15] = __uint_as_float(v[2]) + (a1 ? __uint_as_float(v[3]) : 0.f);
    __syncthreads();
    if (tid < 16) {
      float st = 0.f, ft = 0.f;
#pragma unroll
      for (int w = 0; w < NRED; ++w) { st += s_fin[0][w][tid]; ft += s_fin[1][w][tid]; }
      const float sv = bf16r(st), fv = bf16r(ft);
      const float gv = bf16r(fv / (1.0f + __expf(-fv)));
      const float xo = bf2f(reinterpret_cast<const unsigned short*>(p.x)[b * 16 + tid]);
      const float o = bf16r(xo + bf16r(sv * gv));
      reinterpret_cast<unsigned short*>(p.out)[b * 16 + tid] = (unsigned short)(__float_as_uint(o) >> 16);
    }
  }
}

extern "C" void kernel_launch(void* const* d_in, const int* in_sizes, int n_in,
                              void* d_out, int out_size, void* d_ws, size_t ws_size,
                              hipStream_t stream) {
  Params p;
  p.x = (const bf16*)d_in[0];
  p.kc = (const bf16*)d_in[1];
  p.vc = (const bf16*)d_in[2];
  p.wq = (const bf16*)d_in[3];
  p.wo = (const bf16*)d_in[4];
  p.wg = (const bf16*)d_in[6];
  p.wu = (const bf16*)d_in[7];
  p.wd = (const bf16*)d_in[8];
  p.out = (bf16*)d_out;
  p.gr = (u64*)d_ws;
  hipMemsetAsync(d_ws, 0, GRAN_BYTES, stream);
  fused_kernel<<<NWG, NTH, 0, stream>>>(p);
}
```

```cpp
#include <hip/hip_runtime.h>
#include <hip/hip_bf16.h>
#include <cstdint>

typedef __hip_bfloat16 bf16;
using u32x4 = __attribute__((ext_vector_type(4))) unsigned;
using f32x4 = __attribute__((ext_vector_type(4))) float;
using bf16x2 = __attribute__((ext_vector_type(2))) __bf16;
typedef unsigned long long u64;

constexpr int DM = 576;
constexpr int HKV = 16, SK = 16384, DH = 128, WIN = 4096;
constexpr int NQ = 4096;
constexpr int FF = 4608;
constexpr int NSPLIT = 16;
constexpr int KPS = WIN / NSPLIT;
constexpr int NTH = 576;
constexpr int NWG = 256;
constexpr int NRED = 36;

constexpr int G_Q = 0;
constexpr int G_H = G_Q + NQ;
constexpr int G_PART = G_H + FF;
constexpr int G_SP = G_PART + 32 * NSPLIT * 130;
constexpr int G_END = G_SP + 128 * DM;
constexpr size_t GRAN_BYTES = (size_t)G_END * 8;
static_assert(GRAN_BYTES % 16 == 0, "memset size multiple of 16");

struct Params {
  const bf16* x; const bf16* kc; const bf16* vc; const bf16* wq; const bf16* wo;
  const bf16* wg; const bf16* wu; const bf16* wd; bf16* out; u64* gr;
};

__device__ __forceinline__ float bf2f(unsigned short u) { return __uint_as_float(((unsigned)u) << 16); }
__device__ __forceinline__ float bf16r(float x) {
  unsigned u = __float_as_uint(x);
  u = (u + 0x7FFFu + ((u >> 16) & 1u)) & 0xFFFF0000u;
  return __uint_as_float(u);
}
__device__ __forceinline__ float lo_f(unsigned v) { return __uint_as_float(v << 16); }
__device__ __forceinline__ float hi_f(unsigned v) { return __uint_as_float(v & 0xFFFF0000u); }
__device__ __forceinline__ float dot2(unsigned k, unsigned q, float acc) {
  return __builtin_amdgcn_fdot2_f32_bf16(__builtin_bit_cast(bf16x2, k), __builtin_bit_cast(bf16x2, q), acc, false);
}
template <int CTRL> __device__ __forceinline__ float dpp(float x) {
  return __builtin_bit_cast(float, __builtin_amdgcn_mov_dpp(__builtin_bit_cast(int, x), CTRL, 0xf, 0xf, true));
}
constexpr int XOR1 = 0xB1, XOR2 = 0x4E, XOR7 = 0x141, XOR8 = 0x128, ROR4 = 0x124;
__device__ __forceinline__ float xrow16_max(float x) {
  auto s = __builtin_amdgcn_permlane16_swap(__float_as_uint(x), __float_as_uint(x), false, false);
  x = fmaxf(__uint_as_float(s[0]), __uint_as_float(s[1]));
  auto t = __builtin_amdgcn_permlane32_swap(__float_as_uint(x), __float_as_uint(x), false, false);
  return fmaxf(__uint_as_float(t[0]), __uint_as_float(t[1]));
}
__device__ __forceinline__ float xrow16_sum(float x) {
  auto s = __builtin_amdgcn_permlane16_swap(__float_as_uint(x), __float_as_uint(x), false, false);
  x = __uint_as_float(s[0]) + __uint_as_float(s[1]);
  auto t = __builtin_amdgcn_permlane32_swap(__float_as_uint(x), __float_as_uint(x), false, false);
  return __uint_as_float(t[0]) + __uint_as_float(t[1]);
}
__device__ __forceinline__ float wave_sum(float x) {
  x += dpp<XOR1>(x); x += dpp<XOR2>(x); x += dpp<XOR7>(x); x += dpp<XOR8>(x);
  return xrow16_sum(x);
}
__device__ __forceinline__ u32x4 ldnt(const void* p) { return __builtin_nontemporal_load(reinterpret_cast<const u32x4*>(p)); }

__device__ __forceinline__ void st_gran(u64* g, unsigned v) {
  __hip_atomic_store(g, (1ull << 32) | (u64)v, __ATOMIC_RELAXED, __HIP_MEMORY_SCOPE_AGENT);
}
__device__ __forceinline__ u64 ld_gran(const u64* g) {
  return __hip_atomic_load(g, __ATOMIC_RELAXED, __HIP_MEMORY_SCOPE_AGENT);
}
constexpr int SPIN_CAP = 1 << 22;

__global__ void __launch_bounds__(NTH, 3) fused_kernel(Params p) {
  __shared__ __attribute__((aligned(16))) float s_xn[DM];
  __shared__ __attribute__((aligned(16))) float s_red[32];
  __shared__ __attribute__((aligned(16))) float s_part[2][9][64];
  __shared__ __attribute__((aligned(16))) unsigned short s_q[256];
  __shared__ __attribute__((aligned(16))) float s_p[8 * 64];
  __shared__ __attribute__((aligned(16))) float s_mrg[8 * 2 * (4 + DH)];
  __shared__ __attribute__((aligned(16))) float s_coef[72];
  __shared__ __attribute__((aligned(16))) float s_g2[8 * DM];
  __shared__ __attribute__((aligned(16))) float s_fin[2][NRED][16];

  const int tid = threadIdx.x, lane = tid & 63, wave = __builtin_amdgcn_readfirstlane(tid >> 6);
  const int b = blockIdx.x;
  u64* gr = p.gr;

  const float xv = bf2f(reinterpret_cast<const unsigned short*>(p.x)[tid]);
  const bool isq = b < 64, isgu = (b >= 64) && (b < 136);
  const int cb = isq ? b : b - 64;
  const int rr = lane >> 3, ch = lane & 7;
  u32x4 w1a[8], w1b[8];
  {
    const bf16* pa = isq ? p.wq + (size_t)(wave * 64 + rr) * NQ + cb * 64 + ch * 8
                         : (isgu ? p.wg + (size_t)(wave * 64 + rr) * FF + cb * 64 + ch * 8 : p.x + ch * 8);
    const size_t sa = isq ? (size_t)8 * NQ : (isgu ? (size_t)8 * FF : 0);
    const bf16* pb = isgu ? p.wu + (size_t)(wave * 64 + rr) * FF + cb * 64 + ch * 8 : p.x + ch * 8;
    const size_t sb = isgu ? (size_t)8 * FF : 0;
#pragma unroll
    for (int j = 0; j < 8; ++j) w1a[j] = ldnt(pa + j * sa);
#pragma unroll
    for (int j = 0; j < 8; ++j) w1b[j] = ldnt(pb + j * sb);
  }
  const int g = b >> 4, sp = b & 15;
  const int r = lane >> 4, c = lane & 15;
  u32x4 K[8], V[8];
  {
    const size_t key0 = (size_t)g * SK + (SK - WIN) + sp * KPS + (wave & 7) * 32;
    const bf16* kb = wave < 8 ? p.kc + key0 * DH + r * DH + c * 8 : p.x + ch * 8;
    const bf16* vb = wave < 8 ? p.vc + key0 * DH + r * DH + c * 8 : p.x + ch * 8;
    const int st = wave < 8 ? 4 * DH : 0;
#pragma unroll
    for (int i = 0; i < 8; ++i) K[i] = ldnt(kb + i * st);
#pragma unroll
    for (int i = 0; i < 8; ++i) V[i] = ldnt(vb + i * st);
  }

  {
    float s = wave_sum(xv);
    if (lane == 0) s_red[wave] = s;
    __syncthreads();
    float tot = 0.f;
#pragma unroll
    for (int w = 0; w < 9; ++w) tot += s_red[w];
    const float mean = tot * (1.0f / 576.0f);
    const float dv = xv - mean;
    float s2 = wave_sum(dv * dv);
    if (lane == 0) s_red[16 + wave] = s2;
    __syncthreads();
    float tot2 = 0.f;
#pragma unroll
    for (int w = 0; w < 9; ++w) tot2 += s_red[16 + w];
    const float var = tot2 * (1.0f / 576.0f);
    const float m_bf = bf16r(mean), v_bf = bf16r(var);
    const float den = bf16r(sqrtf(bf16r(v_bf + 1e-5f)));
    s_xn[tid] = bf16r(bf16r(xv - m_bf) / den);
    __syncthreads();
  }

  if (isq || isgu) {
    float acc[8], acb[8];
#pragma unroll
    for (int k = 0; k < 8; ++k) { acc[k] = 0.f; acb[k] = 0.f; }
#pragma unroll
    for (int j = 0; j < 8; ++j) {
      const float xr = s_xn[wave * 64 + j * 8 + rr];
#pragma unroll
      for (int k = 0; k < 4; ++k) {
        acc[2 * k] = fmaf(xr, lo_f(w1a[j][k]), acc[2 * k]);
        acc[2 * k + 1] = fmaf(xr, hi_f(w1a[j][k]), acc[2 * k + 1]);
      }
    }
    if (isgu) {
#pragma unroll
      for (int j = 0; j < 8; ++j) {
        const float xr = s_xn[wave * 64 + j * 8 + rr];
#pragma unroll
        for (int k = 0; k < 4; ++k) {
          acb[2 * k] = fmaf(xr, lo_f(w1b[j][k]), acb[2 * k]);
          acb[2 * k + 1] = fmaf(xr, hi_f(w1b[j][k]), acb[2 * k + 1]);
        }
      }
    }
#pragma unroll
    for (int k = 0; k < 8; ++k) {
      float a = acc[k]; a += dpp<XOR8>(a); acc[k] = xrow16_sum(a);
      float bb = acb[k]; bb += dpp<XOR8>(bb); acb[k] = xrow16_sum(bb);
    }
    if (lane < 8) {
#pragma unroll
      for (int k = 0; k < 8; ++k) { s_part[0][wave][ch * 8 + k] = acc[k]; s_part[1][wave][ch * 8 + k] = acb[k]; }
    }
  }
  __syncthreads();
  if (tid < 64) {
    if (isq) {
      float a = 0.f;
#pragma unroll
      for (int w = 0; w < 9; ++w) a += s_part[0][w][tid];
      st_gran(gr + G_Q + cb * 64 + tid, __float_as_uint(bf16r(a)) >> 16);
    } else if (isgu) {
      float a = 0.f, bb = 0.f;
#pragma unroll
      for (int w = 0; w < 9; ++w) { a += s_part[0][w][tid]; bb += s_part[1][w][tid]; }
      const float gg = bf16r(a), uu = bf16r(bb);
      const float sg = bf16r(gg / (1.0f + __expf(-gg)));
      st_gran(gr + G_H + cb * 64 + tid, __float_as_uint(bf16r(sg * uu)));
    }
  }

  if (wave == 8) {
    const u64* qg = gr + G_Q + g * 256 + lane;
    unsigned qv[4];
    for (int spins = 0; spins < SPIN_CAP; ++spins) {
      bool ok = true;
#pragma unroll
      for (int k = 0; k < 4; ++k) { const u64 x = ld_gran(qg + k * 64); qv[k] = (unsigned)x; ok &= (x >> 32) == 1ull; }
      if (__all(ok)) break;
      __builtin_amdgcn_s_sleep(1);
    }
#pragma unroll
    for (int k = 0; k < 4; ++k) s_q[lane + k * 64] = (unsigned short)qv[k];
  }
  __syncthreads();
  u32x4 w2[9];
  if (wave < 8) {
    u32x4 q[2];
    q[0] = *reinterpret_cast<const u32x4*>(s_q + c * 8);
    q[1] = *reinterpret_cast<const u32x4*>(s_q + DH + c * 8);
    float x[16];
#pragma unroll
    for (int i = 0; i < 8; ++i)
#pragma unroll
      for (int h = 0; h < 2; ++h) {
        float d = 0.f;
#pragma unroll
        for (int k = 0; k < 4; ++k) d = dot2(K[i][k], q[h][k], d);
        x[i * 2 + h] = d;
      }
#define TR_STEP(HALF, CTRL, BIT) _Pragma("unroll") for (int h = 0; h < (HALF); ++h) { \
      const float lo_ = x[h], hi_ = x[h + (HALF)]; const float t1_ = lo_ + dpp<CTRL>(lo_), t2_ = hi_ + dpp<CTRL>(hi_); \
      x[h] = (c & (BIT)) ? t2_ : t1_; }
    TR_STEP(8, XOR8, 8) TR_STEP(4, XOR7, 4) TR_STEP(2, XOR2, 2) TR_STEP(1, XOR1, 1)
#undef TR_STEP
    const float sc = bf16r(bf16r(x[0]) * 0.08838834764831845f);
    float pm = sc;
    pm = fmaxf(pm, dpp<XOR2>(pm));
    pm = fmaxf(pm, dpp<ROR4>(pm));
    pm = fmaxf(pm, dpp<XOR8>(pm));
    pm = xrow16_max(pm);
    const float pr_ = __expf(sc - pm);
    float l = pr_;
    l += dpp<XOR2>(l); l += dpp<ROR4>(l); l += dpp<XOR8>(l); l = xrow16_sum(l);
    float* prow = s_p + (wave * 4 + r) * 16;
    prow[c] = pr_;
    asm volatile("s_waitcnt lgkmcnt(0)" ::: "memory");
    f32x4 pr[4];
#pragma unroll
    for (int u = 0; u < 4; ++u) pr[u] = *reinterpret_cast<const f32x4*>(prow + 4 * u);
    float acc[16];
#pragma unroll
    for (int k = 0; k < 16; ++k) acc[k] = 0.f;
#pragma unroll
    for (int i = 0; i < 8; ++i) {
      float vf[8];
#pragma unroll
      for (int k = 0; k < 4; ++k) { vf[2 * k] = lo_f(V[i][k]); vf[2 * k + 1] = hi_f(V[i][k]); }
#pragma unroll
      for (int h = 0; h < 2; ++h) {
        const float ph = pr[(i * 2 + h) >> 2][(i * 2 + h) & 3];
#pragma unroll
        for (int k = 0; k < 8; ++k) acc[h * 8 + k] = fmaf(ph, vf[k], acc[h * 8 + k]);
      }
    }
#pragma unroll
    for (int k = 0; k < 16; ++k) acc[k] = xrow16_sum(acc[k]);
    if (r == 0) {
#pragma unroll
      for (int h = 0; h < 2; ++h) {
        float* rec = s_mrg + (wave * 2 + h) * (4 + DH);
        *reinterpret_cast<f32x4*>(rec + 4 + 8 * c) = f32x4{acc[h * 8 + 0], acc[h * 8 + 1], acc[h * 8 + 2], acc[h * 8 + 3]};
        *reinterpret_cast<f32x4*>(rec + 4 + 8 * c + 4) = f32x4{acc[h * 8 + 4], acc[h * 8 + 5], acc[h * 8 + 6], acc[h * 8 + 7]};
        if (c == h) { rec[0] = pm; rec[1] = l; }
      }
    }
  }
  const int cg = tid % 72, rp = tid / 72;
  if (b < 64) {
#pragma unroll
    for (int j = 0; j < 8; ++j) w2[j] = ldnt(p.wo + (size_t)(b * 64 + j * 8 + rp) * DM + cg * 8);
  } else if (b < 128) {
#pragma unroll
    for (int j = 0; j < 9; ++j) w2[j] = ldnt(p.wd + (size_t)((b - 64) * 72 + j * 8 + rp) * DM + cg * 8);
  }
  __syncthreads();
  if (tid < 256) {
    const int h = tid >> 7, d = tid & 127;
    float M = -INFINITY;
#pragma unroll
    for (int w = 0; w < 8; ++w) M = fmaxf(M, s_mrg[(w * 2 + h) * (4 + DH)]);
    float L = 0.f, o = 0.f;
#pragma unroll
    for (int w = 0; w < 8; ++w) {
      const float* rec = s_mrg + (w * 2 + h) * (4 + DH);
      const float wt = __expf(rec[0] - M);
      L = fmaf(wt, rec[1], L);
      o = fmaf(wt, rec[4 + d], o);
    }
    u64* pg = gr + G_PART + ((g * 2 + h) * NSPLIT + sp) * 130;
    st_gran(pg + 2 + d, __float_as_uint(o));
    if (d == 0) { st_gran(pg, __float_as_uint(M)); st_gran(pg + 1, __float_as_uint(L)); }
  }

  if (b < 128) {
    if (wave == 8) {
      if (b < 64) {
        const int head = b >> 1, d = (b & 1) * 64 + lane;
        const u64* pg = gr + G_PART + head * NSPLIT * 130;
        unsigned mv[NSPLIT], lv[NSPLIT], ov[NSPLIT];
        for (int spins = 0; spins < SPIN_CAP; ++spins) {
          bool ok = true;
#pragma unroll
          for (int s = 0; s < NSPLIT; ++s) {
            const u64 a = ld_gran(pg + s * 130), bq = ld_gran(pg + s * 130 + 1), cq = ld_gran(pg + s * 130 + 2 + d);
            mv[s] = (unsigned)a; lv[s] = (unsigned)bq; ov[s] = (unsigned)cq;
            ok &= ((a >> 32) == 1ull) & ((bq >> 32) == 1ull) & ((cq >> 32) == 1ull);
          }
          if (__all(ok)) break;
          __builtin_amdgcn_s_sleep(1);
        }
        float M = -INFINITY;
#pragma unroll
        for (int s = 0; s < NSPLIT; ++s) M = fmaxf(M, __uint_as_float(mv[s]));
        float L = 0.f, o = 0.f;
#pragma unroll
        for (int s = 0; s < NSPLIT; ++s) {
          const float wt = __expf(__uint_as_float(mv[s]) - M);
          L = fmaf(wt, __uint_as_float(lv[s]), L);
          o = fmaf(wt, __uint_as_float(ov[s]), o);
        }
        s_coef[lane] = bf16r(o / L);
      } else {
        const u64* hg = gr + G_H + (b - 64) * 72;
        unsigned hv[2];
        for (int spins = 0; spins < SPIN_CAP; ++spins) {
          const u64 a = ld_gran(hg + lane);
          const u64 bq = ld_gran(hg + 64 + (lane & 7));
          hv[0] = (unsigned)a; hv[1] = (unsigned)bq;
          const bool ok = ((a >> 32) == 1ull) & ((bq >> 32) == 1ull);
          if (__all(ok)) break;
          __builtin_amdgcn_s_sleep(1);
        }
        s_coef[lane] = __uint_as_float(hv[0]);
        if (lane < 8) s_coef[64 + lane] = __uint_as_float(hv[1]);
      }
    }
    __syncthreads();
    float acc[8];
#pragma unroll
    for (int k = 0; k < 8; ++k) acc[k] = 0.f;
#pragma unroll
    for (int j = 0; j < 9; ++j) {
      if (j < 8 || b >= 64) {
        const float hv = s_coef[j * 8 + rp];
#pragma unroll
        for (int k = 0; k < 4; ++k) {
          acc[2 * k] = fmaf(hv, lo_f(w2[j][k]), acc[2 * k]);
          acc[2 * k + 1] = fmaf(hv, hi_f(w2[j][k]), acc[2 * k + 1]);
        }
      }
    }
    *reinterpret_cast<f32x4*>(s_g2 + rp * DM + cg * 8) = f32x4{acc[0], acc[1], acc[2], acc[3]};
    *reinterpret_cast<f32x4*>(s_g2 + rp * DM + cg * 8 + 4) = f32x4{acc[4], acc[5], acc[6], acc[7]};
    __syncthreads();
    float s = 0.f;
#pragma unroll
    for (int rq = 0; rq < 8; ++rq) s += s_g2[rq * DM + tid];
    st_gran(gr + G_SP + b * DM + tid, __float_as_uint(s));
  }

  if (b < NRED) {
    const int col = b * 16 + (tid & 15), grp = tid >> 4;
    const u64* sg = gr + G_SP + col;
    unsigned v[4];
    const bool a1 = (grp + 36) < 64;
    for (int spins = 0; spins < SPIN_CAP; ++spins) {
      bool ok = true;
      const u64 x0 = ld_gran(sg + (size_t)grp * DM);
      const u64 x1 = ld_gran(sg + (size_t)(a1 ? grp + 36 : grp) * DM);
      const u64 x2 = ld_gran(sg + (size_t)(64 + grp) * DM);
      const u64 x3 = ld_gran(sg + (size_t)(64 + (a1 ? grp + 36 : grp)) * DM);
      v[0] = (unsigned)x0; v[1] = (unsigned)x1; v[2] = (unsigned)x2; v[3] = (unsigned)x3;
      ok = ((x0 >> 32) == 1ull) & ((x1 >> 32) == 1ull) & ((x2 >> 32) == 1ull) & ((x3 >> 32) == 1ull);
      if (__all(ok)) break;
      __builtin_amdgcn_s_sleep(1);
    }
    s_fin[0][grp][tid & 15] = __uint_as_float(v[0]) + (a1 ? __uint_as_float(v[1]) : 0.f);
    s_fin[1][grp][tid & 15] = __uint_as_float(v[2]) + (a1 ? __uint_as_float(v[3]) : 0.f);
    __syncthreads();
    if (tid < 16) {
      float st = 0.f, ft = 0.f;
#pragma unroll
      for (int w = 0; w < NRED; ++w) { st += s_fin[0][w][tid]; ft += s_fin[1][w][tid]; }
      const float sv = bf16r(st), fv = bf16r(ft);
      const float gv = bf16r(fv / (1.0f + __expf(-fv)));
      const float xo = bf2f(reinterpret_cast<const unsigned short*>(p.x)[b * 16 + tid]);
      const float o = bf16r(xo + bf16r(sv * gv));
      reinterpret_cast<unsigned short*>(p.out)[b * 16 + tid] = (unsigned short)(__float_as_uint(o) >> 16);
    }
  }
}

extern "C" void kernel_launch(void* const* d_in, const int* in_sizes, int n_in,
                              void* d_out, int out_size, void* d_ws, size_t ws_size,
                              hipStream_t stream) {
  Params p;
  p.x = (const bf16*)d_in[0];
  p.kc = (const bf16*)d_in[1];
  p.vc = (const bf16*)d_in[2];
  p.wq = (const bf16*)d_in[3];
  p.wo = (const bf16*)d_in[4];
  p.wg = (const bf16*)d_in[6];
  p.wu = (const bf16*)d_in[7];
  p.wd = (const bf16*)d_in[8];
  p.out = (bf16*)d_out;
  p.gr = (u64*)d_ws;
  hipMemsetAsync(d_ws, 0, GRAN_BYTES, stream);
  fused_kernel<<<NWG, NTH, 0, stream>>>(p);
}
```

```cpp
#include <hip/hip_runtime.h>
#include <hip/hip_bf16.h>
#include <cstdint>

typedef __hip_bfloat16 bf16;
using u32x4 = __attribute__((ext_vector_type(4))) unsigned;
using f32x4 = __attribute__((ext_vector_type(4))) float;
using bf16x2 = __attribute__((ext_vector_type(2))) __bf16;
typedef unsigned long long u64;

constexpr int DM = 576;
constexpr int HKV = 16, SK = 16384, DH = 128, WIN = 4096;
constexpr int NQ = 4096;
constexpr int FF = 4608;
constexpr int NSPLIT = 16;
constexpr int KPS = WIN / NSPLIT;
constexpr int NTH = 1024;
constexpr int NWG = 256;
constexpr int NRED = 36;

constexpr int G_Q = 0;
constexpr int G_H = G_Q + NQ;
constexpr int G_PART = G_H + FF;
constexpr int G_SP = G_PART + 32 * NSPLIT * 130;
constexpr int G_END = G_SP + 128 * DM;
constexpr size_t GRAN_BYTES = (size_t)G_END * 8;
static_assert(GRAN_BYTES % 16 == 0, "memset size multiple of 16");

struct Params {
  const bf16* x; const bf16* kc; const bf16* vc; const bf16* wq; const bf16* wo;
  const bf16* wg; const bf16* wu; const bf16* wd; bf16* out; u64* gr;
};

__device__ __forceinline__ float bf2f(unsigned short u) { return __uint_as_float(((unsigned)u) << 16); }
__device__ __forceinline__ float bf16r(float x) {
  unsigned u = __float_as_uint(x);
  u = (u + 0x7FFFu + ((u >> 16) & 1u)) & 0xFFFF0000u;
  return __uint_as_float(u);
}
__device__ __forceinline__ float lo_f(unsigned v) { return __uint_as_float(v << 16); }
__device__ __forceinline__ float hi_f(unsigned v) { return __uint_as_float(v & 0xFFFF0000u); }
__device__ __forceinline__ float dot2(unsigned k, unsigned q, float acc) {
  return __builtin_amdgcn_fdot2_f32_bf16(__builtin_bit_cast(bf16x2, k), __builtin_bit_cast(bf16x2, q), acc, false);
}
template <int CTRL> __device__ __forceinline__ float dpp(float x) {
  return __builtin_bit_cast(float, __builtin_amdgcn_mov_dpp(__builtin_bit_cast(int, x), CTRL, 0xf, 0xf, true));
}
constexpr int XOR1 = 0xB1, XOR2 = 0x4E, XOR7 = 0x141, XOR8 = 0x128, ROR4 = 0x124;
__device__ __forceinline__ float xrow16_max(float x) {
  auto s = __builtin_amdgcn_permlane16_swap(__float_as_uint(x), __float_as_uint(x), false, false);
  x = fmaxf(__uint_as_float(s[0]), __uint_as_float(s[1]));
  auto t = __builtin_amdgcn_permlane32_swap(__float_as_uint(x), __float_as_uint(x), false, false);
  return fmaxf(__uint_as_float(t[0]), __uint_as_float(t[1]));
}
__device__ __forceinline__ float xrow16_sum(float x) {
  auto s = __builtin_amdgcn_permlane16_swap(__float_as_uint(x), __float_as_uint(x), false, false);
  x = __uint_as_float(s[0]) + __uint_as_float(s[1]);
  auto t = __builtin_amdgcn_permlane32_swap(__float_as_uint(x), __float_as_uint(x), false, false);
  return __uint_as_float(t[0]) + __uint_as_float(t[1]);
}
__device__ __forceinline__ float wave_sum(float x) {
  x += dpp<XOR1>(x); x += dpp<XOR2>(x); x += dpp<XOR7>(x); x += dpp<XOR8>(x);
  return xrow16_sum(x);
}
__device__ __forceinline__ u32x4 ldnt(const void* p) { return __builtin_nontemporal_load(reinterpret_cast<const u32x4*>(p)); }

__device__ __forceinline__ void st_gran(u64* g, unsigned v) {
  __hip_atomic_store(g, (1ull << 32) | (u64)v, __ATOMIC_RELAXED, __HIP_MEMORY_SCOPE_AGENT);
}
__device__ __forceinline__ u64 ld_gran(const u64* g) {
  return __hip_atomic_load(g, __ATOMIC_RELAXED, __HIP_MEMORY_SCOPE_AGENT);
}
constexpr int SPIN_CAP = 1 << 17;

__device__ __forceinline__ unsigned lds_arrive(unsigned* cnt) {
  return __hip_atomic_fetch_add(cnt, 1u, __ATOMIC_ACQ_REL, __HIP_MEMORY_SCOPE_WORKGROUP);
}
__device__ __forceinline__ void lds_set(unsigned* flag) {
  __hip_atomic_store(flag, 1u, __ATOMIC_RELEASE, __HIP_MEMORY_SCOPE_WORKGROUP);
}
__device__ __forceinline__ void lds_wait(unsigned* flag) {
  for (int spins = 0; spins < (1 << 24); ++spins) {
    if (__hip_atomic_load(flag, __ATOMIC_ACQUIRE, __HIP_MEMORY_SCOPE_WORKGROUP) != 0u) break;
    __builtin_amdgcn_s_sleep(1);
  }
}

#define STAMP(i) do {} while (0)

__global__ void __launch_bounds__(NTH, 4) fused_kernel(Params p) {
  __shared__ __attribute__((aligned(16))) float s_xn[DM];
  __shared__ __attribute__((aligned(16))) float s_part[2][8][64];
  __shared__ __attribute__((aligned(16))) unsigned short s_q[256];
  __shared__ __attribute__((aligned(16))) float s_p[8 * 64];
  __shared__ __attribute__((aligned(16))) float s_mrg[8 * 2 * (4 + DH)];
  __shared__ __attribute__((aligned(16))) float s_coef[72];
  __shared__ __attribute__((aligned(16))) float s_g2[8 * DM];
  __shared__ __attribute__((aligned(16))) float s_fin[2][32][16];
  __shared__ __attribute__((aligned(16))) unsigned s_sync[16];

  const int tid = threadIdx.x, lane = tid & 63, wave = __builtin_amdgcn_readfirstlane(tid >> 6);
  const int b = blockIdx.x;
  u64* gr = p.gr;
  if (tid < 16) s_sync[tid] = 0u;
  __syncthreads();
  STAMP(0);

  if (wave < 8) {
    const int g = b >> 4, sp = b & 15;
    const int r = lane >> 4, c = lane & 15;
    u32x4 K[8], V[8];
    {
      const size_t key0 = (size_t)g * SK + (SK - WIN) + sp * KPS + wave * 32;
      const bf16* kb = p.kc + key0 * DH + r * DH + c * 8;
      const bf16* vb = p.vc + key0 * DH + r * DH + c * 8;
#pragma unroll
      for (int i = 0; i < 8; ++i) K[i] = ldnt(kb + i * 4 * DH);
#pragma unroll
      for (int i = 0; i < 8; ++i) V[i] = ldnt(vb + i * 4 * DH);
    }
    lds_wait(&s_sync[1]);
    STAMP(3);
    u32x4 q[2];
    q[0] = *reinterpret_cast<const u32x4*>(s_q + c * 8);
    q[1] = *reinterpret_cast<const u32x4*>(s_q + DH + c * 8);
    float x[16];
#pragma unroll
    for (int i = 0; i < 8; ++i)
#pragma unroll
      for (int h = 0; h < 2; ++h) {
        float d = 0.f;
#pragma unroll
        for (int k = 0; k < 4; ++k) d = dot2(K[i][k], q[h][k], d);
        x[i * 2 + h] = d;
      }
#define TR_STEP(HALF, CTRL, BIT) _Pragma("unroll") for (int h = 0; h < (HALF); ++h) { \
      const float lo_ = x[h], hi_ = x[h + (HALF)]; const float t1_ = lo_ + dpp<CTRL>(lo_), t2_ = hi_ + dpp<CTRL>(hi_); \
      x[h] = (c & (BIT)) ? t2_ : t1_; }
    TR_STEP(8, XOR8, 8) TR_STEP(4, XOR7, 4) TR_STEP(2, XOR2, 2) TR_STEP(1, XOR1, 1)
#undef TR_STEP
    const float sc = bf16r(bf16r(x[0]) * 0.08838834764831845f);
    float pm = sc;
    pm = fmaxf(pm, dpp<XOR2>(pm));
    pm = fmaxf(pm, dpp<ROR4>(pm));
    pm = fmaxf(pm, dpp<XOR8>(pm));
    pm = xrow16_max(pm);
    const float pr_ = __expf(sc - pm);
    float l = pr_;
    l += dpp<XOR2>(l); l += dpp<ROR4>(l); l += dpp<XOR8>(l); l = xrow16_sum(l);
    float* prow = s_p + (wave * 4 + r) * 16;
    prow[c] = pr_;
    asm volatile("s_waitcnt lgkmcnt(0)" ::: "memory");
    f32x4 pr[4];
#pragma unroll
    for (int u = 0; u < 4; ++u) pr[u] = *reinterpret_cast<const f32x4*>(prow + 4 * u);
    float acc[16];
#pragma unroll
    for (int k = 0; k < 16; ++k) acc[k] = 0.f;
#pragma unroll
    for (int i = 0; i < 8; ++i) {
      float vf[8];
#pragma unroll
      for (int k = 0; k < 4; ++k) { vf[2 * k] = lo_f(V[i][k]); vf[2 * k + 1] = hi_f(V[i][k]); }
#pragma unroll
      for (int h = 0; h < 2; ++h) {
        const float ph = pr[(i * 2 + h) >> 2][(i * 2 + h) & 3];
#pragma unroll
        for (int k = 0; k < 8; ++k) acc[h * 8 + k] = fmaf(ph, vf[k], acc[h * 8 + k]);
      }
    }
#pragma unroll
    for (int k = 0; k < 16; ++k) acc[k] = xrow16_sum(acc[k]);
    if (r == 0) {
#pragma unroll
      for (int h = 0; h < 2; ++h) {
        float* rec = s_mrg + (wave * 2 + h) * (4 + DH);
        *reinterpret_cast<f32x4*>(rec + 4 + 8 * c) = f32x4{acc[h * 8 + 0], acc[h * 8 + 1], acc[h * 8 + 2], acc[h * 8 + 3]};
        *reinterpret_cast<f32x4*>(rec + 4 + 8 * c + 4) = f32x4{acc[h * 8 + 4], acc[h * 8 + 5], acc[h * 8 + 6], acc[h * 8 + 7]};
        if (c == h) { rec[0] = pm; rec[1] = l; }
      }
    }
    unsigned prev = 0;
    if (lane == 0) prev = lds_arrive(&s_sync[2]);
    prev = __builtin_amdgcn_readfirstlane(prev);
    if (prev == 7u) {
#pragma unroll
      for (int h = 0; h < 2; ++h) {
        float M = -INFINITY;
#pragma unroll
        for (int w = 0; w < 8; ++w) M = fmaxf(M, s_mrg[(w * 2 + h) * (4 + DH)]);
        float L = 0.f, o0 = 0.f, o1 = 0.f;
#pragma unroll
        for (int w = 0; w < 8; ++w) {
          const float* rec = s_mrg + (w * 2 + h) * (4 + DH);
          const float wt = __expf(rec[0] - M);
          L = fmaf(wt, rec[1], L);
          o0 = fmaf(wt, rec[4 + lane], o0);
          o1 = fmaf(wt, rec[4 + 64 + lane], o1);
        }
        u64* pg = gr + G_PART + ((g * 2 + h) * NSPLIT + sp) * 130;
        st_gran(pg + 2 + lane, __float_as_uint(o0));
        st_gran(pg + 2 + 64 + lane, __float_as_uint(o1));
        if (lane == 0) { st_gran(pg, __float_as_uint(M)); st_gran(pg + 1, __float_as_uint(L)); }
      }
    }
    STAMP(4);
    return;
  }

  const int gw = wave - 8;
  const bool isq = b < 64, isgu = (b >= 64) && (b < 136);
  const int cb = isq ? b : b - 64;
  const int rr = lane >> 3, ch = lane & 7;
  float xv[9];
#pragma unroll
  for (int k = 0; k < 9; ++k) xv[k] = bf2f(reinterpret_cast<const unsigned short*>(p.x)[lane + 64 * k]);
  u32x4 w1a[9], w1b[9];
  {
    const bf16* pa = isq ? p.wq + (size_t)(gw * 72 + rr) * NQ + cb * 64 + ch * 8
                         : (isgu ? p.wg + (size_t)(gw * 72 + rr) * FF + cb * 64 + ch * 8 : p.x + ch * 8);
    const size_t sa = isq ? (size_t)8 * NQ : (isgu ? (size_t)8 * FF : 0);
    const bf16* pb = isgu ? p.wu + (size_t)(gw * 72 + rr) * FF + cb * 64 + ch * 8 : p.x + ch * 8;
    const size_t sb = isgu ? (size_t)8 * FF : 0;
#pragma unroll
    for (int j = 0; j < 9; ++j) w1a[j] = ldnt(pa + j * sa);
#pragma unroll
    for (int j = 0; j < 9; ++j) w1b[j] = ldnt(pb + j * sb);
  }
  {
    float s = 0.f;
#pragma unroll
    for (int k = 0; k < 9; ++k) s += xv[k];
    const float mean = wave_sum(s) * (1.0f / 576.0f);
    float s2 = 0.f;
#pragma unroll
    for (int k = 0; k < 9; ++k) { const float dv = xv[k] - mean; s2 = fmaf(dv, dv, s2); }
    const float var = wave_sum(s2) * (1.0f / 576.0f);
    const float m_bf = bf16r(mean), v_bf = bf16r(var);
    const float den = bf16r(sqrtf(bf16r(v_bf + 1e-5f)));
#pragma unroll
    for (int k = 0; k < 9; ++k) s_xn[lane + 64 * k] = bf16r(bf16r(xv[k] - m_bf) / den);
    asm volatile("s_waitcnt lgkmcnt(0)" ::: "memory");
  }
  STAMP(1);
  {
    float acc[8], acb[8];
#pragma unroll
    for (int k = 0; k < 8; ++k) { acc[k] = 0.f; acb[k] = 0.f; }
#pragma unroll
    for (int j = 0; j < 9; ++j) {
      const float xr = s_xn[gw * 72 + j * 8 + rr];
#pragma unroll
      for (int k = 0; k < 4; ++k) {
        acc[2 * k] = fmaf(xr, lo_f(w1a[j][k]), acc[2 * k]);
        acc[2 * k + 1] = fmaf(xr, hi_f(w1a[j][k]), acc[2 * k + 1]);
        acb[2 * k] = fmaf(xr, lo_f(w1b[j][k]), acb[2 * k]);
        acb[2 * k + 1] = fmaf(xr, hi_f(w1b[j][k]), acb[2 * k + 1]);
      }
    }
#pragma unroll
    for (int k = 0; k < 8; ++k) {
      float a = acc[k]; a += dpp<XOR8>(a); acc[k] = xrow16_sum(a);
      float bb = acb[k]; bb += dpp<XOR8>(bb); acb[k] = xrow16_sum(bb);
    }
    if (lane < 8) {
#pragma unroll
      for (int k = 0; k < 8; ++k) { s_part[0][gw][ch * 8 + k] = acc[k]; s_part[1][gw][ch * 8 + k] = acb[k]; }
    }
    unsigned prev = 0;
    if (lane == 0) prev = lds_arrive(&s_sync[0]);
    prev = __builtin_amdgcn_readfirstlane(prev);
    if (prev == 7u) {
      if (isq) {
        float a = 0.f;
#pragma unroll
        for (int w = 0; w < 8; ++w) a += s_part[0][w][lane];
        st_gran(gr + G_Q + cb * 64 + lane, __float_as_uint(bf16r(a)) >> 16);
      } else if (isgu) {
        float a = 0.f, bb = 0.f;
#pragma unroll
        for (int w = 0; w < 8; ++w) { a += s_part[0][w][lane]; bb += s_part[1][w][lane]; }
        const float gg = bf16r(a), uu = bf16r(bb);
        const float sg = bf16r(gg / (1.0f + __expf(-gg)));
        st_gran(gr + G_H + cb * 64 + lane, __float_as_uint(bf16r(sg * uu)));
      }
    }
  }
  STAMP(2);
  if (gw == 0) {
    const u64* qg = gr + G_Q + (b >> 4) * 256 + lane;
    unsigned qv[4];
    for (int spins = 0; spins < SPIN_CAP; ++spins) {
      bool ok = true;
#pragma unroll
      for (int k = 0; k < 4; ++k) { const u64 x = ld_gran(qg + k * 64); qv[k] = (unsigned)x; ok &= (x >> 32) == 1ull; }
      if (__all(ok)) break;
      __builtin_amdgcn_s_sleep(1);
    }
#pragma unroll
    for (int k = 0; k < 4; ++k) s_q[lane + k * 64] = (unsigned short)qv[k];
    if (lane == 0) lds_set(&s_sync[1]);
  }
  if (b >= 128) return;

  const int nrows = (b < 64) ? 8 : 9;
  u32x4 w2[9], w2x[2];
  {
    const bf16* slab = (b < 64) ? p.wo + (size_t)b * 64 * DM : p.wd + (size_t)(b - 64) * 72 * DM;
    const bf16* pm = slab + (size_t)gw * DM + lane * 8;
#pragma unroll
    for (int j = 0; j < 9; ++j) { const int jj = j < nrows ? j : 0; w2[j] = ldnt(pm + (size_t)jj * 8 * DM); }
    w2x[0] = ldnt(slab + (size_t)(gw * 8 + (lane >> 3)) * DM + 512 + (lane & 7) * 8);
    w2x[1] = ldnt(slab + (size_t)((b < 64) ? 0 : 64 + gw) * DM + 512 + (lane & 7) * 8);
  }
  if (gw == 0) {
    if (b < 64) {
      const int head = b >> 1, d = (b & 1) * 64 + lane;
      const u64* pg = gr + G_PART + head * NSPLIT * 130;
      unsigned mv = 0, lv = 0, ov[NSPLIT];
      for (int spins = 0; spins < SPIN_CAP; ++spins) {
        const u64 a = ld_gran(pg + (lane & 15) * 130), bq = ld_gran(pg + (lane & 15) * 130 + 1);
        mv = (unsigned)a; lv = (unsigned)bq;
        bool ok = ((a >> 32) == 1ull) & ((bq >> 32) == 1ull);
#pragma unroll
        for (int s = 0; s < NSPLIT; ++s) {
          const u64 cq = ld_gran(pg + s * 130 + 2 + d);
          ov[s] = (unsigned)cq;
          ok &= ((cq >> 32) == 1ull);
        }
        if (__all(ok)) break;
        __builtin_amdgcn_s_sleep(1);
      }
      const float ms = __uint_as_float(mv);
      float M = ms;
      M = fmaxf(M, dpp<XOR1>(M)); M = fmaxf(M, dpp<XOR2>(M)); M = fmaxf(M, dpp<XOR7>(M)); M = fmaxf(M, dpp<XOR8>(M));
      const float wt = __expf(ms - M);
      float L = wt * __uint_as_float(lv);
      L += dpp<XOR1>(L); L += dpp<XOR2>(L); L += dpp<XOR7>(L); L += dpp<XOR8>(L);
      float o = 0.f;
#pragma unroll
      for (int s = 0; s < NSPLIT; ++s) {
        const float ws_ = __uint_as_float(__builtin_amdgcn_readlane(__float_as_uint(wt), s));
        o = fmaf(ws_, __uint_as_float(ov[s]), o);
      }
      s_coef[lane] = bf16r(o / L);
    } else {
      const u64* hg = gr + G_H + (b - 64) * 72;
      unsigned hv[2];
      for (int spins = 0; spins < SPIN_CAP; ++spins) {
        const u64 a = ld_gran(hg + lane);
        const u64 bq = ld_gran(hg + 64 + (lane & 7));
        hv[0] = (unsigned)a; hv[1] = (unsigned)bq;
        const bool ok = ((a >> 32) == 1ull) & ((bq >> 32) == 1ull);
        if (__all(ok)) break;
        __builtin_amdgcn_s_sleep(1);
      }
      s_coef[lane] = __uint_as_float(hv[0]);
      if (lane < 8) s_coef[64 + lane] = __uint_as_float(hv[1]);
    }
    if (lane == 0) lds_set(&s_sync[3]);
  } else {
    lds_wait(&s_sync[3]);
  }
  STAMP(5);
  {
    float acc[8], acx[8];
#pragma unroll
    for (int k = 0; k < 8; ++k) { acc[k] = 0.f; acx[k] = 0.f; }
#pragma unroll
    for (int j = 0; j < 9; ++j) {
      const float hv = (j < nrows) ? s_coef[j * 8 + gw] : 0.f;
#pragma unroll
      for (int k = 0; k < 4; ++k) {
        acc[2 * k] = fmaf(hv, lo_f(w2[j][k]), acc[2 * k]);
        acc[2 * k + 1] = fmaf(hv, hi_f(w2[j][k]), acc[2 * k + 1]);
      }
    }
    {
      const float h0 = s_coef[gw * 8 + (lane >> 3)];
      const float h1 = (b >= 64 && lane < 8) ? s_coef[64 + gw] : 0.f;
#pragma unroll
      for (int k = 0; k < 4; ++k) {
        acx[2 * k] = fmaf(h1, lo_f(w2x[1][k]), h0 * lo_f(w2x[0][k]));
        acx[2 * k + 1] = fmaf(h1, hi_f(w2x[1][k]), h0 * hi_f(w2x[0][k]));
      }
#pragma unroll
      for (int k = 0; k < 8; ++k) { float a = acx[k]; a += dpp<XOR8>(a); acx[k] = xrow16_sum(a); }
    }
    *reinterpret_cast<f32x4*>(s_g2 + gw * DM + lane * 8) = f32x4{acc[0], acc[1], acc[2], acc[3]};
    *reinterpret_cast<f32x4*>(s_g2 + gw * DM + lane * 8 + 4) = f32x4{acc[4], acc[5], acc[6], acc[7]};
    if (lane < 8) {
      *reinterpret_cast<f32x4*>(s_g2 + gw * DM + 512 + lane * 8) = f32x4{acx[0], acx[1], acx[2], acx[3]};
      *reinterpret_cast<f32x4*>(s_g2 + gw * DM + 512 + lane * 8 + 4) = f32x4{acx[4], acx[5], acx[6], acx[7]};
    }
    unsigned prev = 0;
    if (lane == 0) prev = lds_arrive(&s_sync[4]);
    prev = __builtin_amdgcn_readfirstlane(prev);
    if (prev == 7u) {
#pragma unroll
      for (int k = 0; k < 9; ++k) {
        float s = 0.f;
#pragma unroll
        for (int w = 0; w < 8; ++w) s += s_g2[w * DM + lane + 64 * k];
        st_gran(gr + G_SP + b * DM + lane + 64 * k, __float_as_uint(s));
      }
    }
  }
  STAMP(6);
  if (b >= NRED) return;

  {
    const int t = tid - 512;
    const int col = b * 16 + (t & 15), grp = t >> 4;
    const u64* sg = gr + G_SP + col;
    unsigned v[4];
    for (int spins = 0; spins < SPIN_CAP; ++spins) {
      const u64 x0 = ld_gran(sg + (size_t)grp * DM);
      const u64 x1 = ld_gran(sg + (size_t)(grp + 32) * DM);
      const u64 x2 = ld_gran(sg + (size_t)(64 + grp) * DM);
      const u64 x3 = ld_gran(sg + (size_t)(96 + grp) * DM);
      v[0] = (unsigned)x0; v[1] = (unsigned)x1; v[2] = (unsigned)x2; v[3] = (unsigned)x3;
      const bool ok = ((x0 >> 32) == 1ull) & ((x1 >> 32) == 1ull) & ((x2 >> 32) == 1ull) & ((x3 >> 32) == 1ull);
      if (__all(ok)) break;
      __builtin_amdgcn_s_sleep(1);
    }
    s_fin[0][grp][t & 15] = __uint_as_float(v[0]) + __uint_as_float(v[1]);
    s_fin[1][grp][t & 15] = __uint_as_float(v[2]) + __uint_as_float(v[3]);
    unsigned prev = 0;
    if (lane == 0) prev = lds_arrive(&s_sync[5]);
    prev = __builtin_amdgcn_readfirstlane(prev);
    STAMP(7);
    if (prev == 7u && lane < 16) {
      float st = 0.f, ft = 0.f;
#pragma unroll
      for (int w = 0; w < 32; ++w) { st += s_fin[0][w][lane]; ft += s_fin[1][w][lane]; }
      const float sv = bf16r(st), fv = bf16r(ft);
      const float gv = bf16r(fv / (1.0f + __expf(-fv)));
      const float xo = bf2f(reinterpret_cast<const unsigned short*>(p.x)[b * 16 + lane]);
      const float o = bf16r(xo + bf16r(sv * gv));
      reinterpret_cast<unsigned short*>(p.out)[b * 16 + lane] = (unsigned short)(__float_as_uint(o) >> 16);
    }
  }
}

extern "C" void kernel_launch(void* const* d_in, const int* in_sizes, int n_in,
                              void* d_out, int out_size, void* d_ws, size_t ws_size,
                              hipStream_t stream) {
  Params p;
  p.x = (const bf16*)d_in[0];
  p.kc = (const bf16*)d_in[1];
  p.vc = (const bf16*)d_in[2];
  p.wq = (const bf16*)d_in[3];
  p.wo = (const bf16*)d_in[4];
  p.wg = (const bf16*)d_in[6];
  p.wu = (const bf16*)d_in[7];
  p.wd = (const bf16*)d_in[8];
  p.out = (bf16*)d_out;
  p.gr = (u64*)d_ws;
  (void)hipMemsetAsync(d_ws, 0, GRAN_BYTES, stream);
  fused_kernel<<<NWG, NTH, 0, stream>>>(p);
}
```

```cpp
#include <hip/hip_runtime.h>
#include <hip/hip_bf16.h>
#include <cstdint>

typedef __hip_bfloat16 bf16;
using u32x4 = __attribute__((ext_vector_type(4))) unsigned;
using f32x4 = __attribute__((ext_vector_type(4))) float;
using bf16x2 = __attribute__((ext_vector_type(2))) __bf16;
typedef unsigned long long u64;

constexpr int DM = 576;
constexpr int HKV = 16, SK = 16384, DH = 128, WIN = 4096;
constexpr int NQ = 4096;
constexpr int FF = 4608;
constexpr int NSPLIT = 16;
constexpr int KPS = WIN / NSPLIT;
constexpr int NTH = 1024;
constexpr int NWG = 256;
constexpr int NRED = 36;

constexpr int G_Q = 0;
constexpr int G_H = G_Q + NQ;
constexpr int G_PART = G_H + FF;
constexpr int G_SP = G_PART + 32 * NSPLIT * 65;
constexpr int G_END = G_SP + 128 * 320;
constexpr size_t GRAN_BYTES = (size_t)G_END * 8;
static_assert(GRAN_BYTES % 16 == 0, "memset size multiple of 16");

struct Params {
  const bf16* x; const bf16* kc; const bf16* vc; const bf16* wq; const bf16* wo;
  const bf16* wg; const bf16* wu; const bf16* wd; bf16* out; u64* gr;
};

__device__ __forceinline__ float bf2f(unsigned short u) { return __uint_as_float(((unsigned)u) << 16); }
__device__ __forceinline__ float bf16r(float x) {
  unsigned u = __float_as_uint(x);
  u = (u + 0x7FFFu + ((u >> 16) & 1u)) & 0xFFFF0000u;
  return __uint_as_float(u);
}
__device__ __forceinline__ float lo_f(unsigned v) { return __uint_as_float(v << 16); }
__device__ __forceinline__ float hi_f(unsigned v) { return __uint_as_float(v & 0xFFFF0000u); }
__device__ __forceinline__ float dot2(unsigned k, unsigned q, float acc) {
  return __builtin_amdgcn_fdot2_f32_bf16(__builtin_bit_cast(bf16x2, k), __builtin_bit_cast(bf16x2, q), acc, false);
}
template <int CTRL> __device__ __forceinline__ float dpp(float x) {
  return __builtin_bit_cast(float, __builtin_amdgcn_mov_dpp(__builtin_bit_cast(int, x), CTRL, 0xf, 0xf, true));
}
constexpr int XOR1 = 0xB1, XOR2 = 0x4E, XOR7 = 0x141, XOR8 = 0x128, ROR4 = 0x124;
__device__ __forceinline__ float xrow16_max(float x) {
  auto s = __builtin_amdgcn_permlane16_swap(__float_as_uint(x), __float_as_uint(x), false, false);
  x = fmaxf(__uint_as_float(s[0]), __uint_as_float(s[1]));
  auto t = __builtin_amdgcn_permlane32_swap(__float_as_uint(x), __float_as_uint(x), false, false);
  return fmaxf(__uint_as_float(t[0]), __uint_as_float(t[1]));
}
__device__ __forceinline__ float xrow16_sum(float x) {
  auto s = __builtin_amdgcn_permlane16_swap(__float_as_uint(x), __float_as_uint(x), false, false);
  x = __uint_as_float(s[0]) + __uint_as_float(s[1]);
  auto t = __builtin_amdgcn_permlane32_swap(__float_as_uint(x), __float_as_uint(x), false, false);
  return __uint_as_float(t[0]) + __uint_as_float(t[1]);
}
__device__ __forceinline__ float wave_sum(float x) {
  x += dpp<XOR1>(x); x += dpp<XOR2>(x); x += dpp<XOR7>(x); x += dpp<XOR8>(x);
  return xrow16_sum(x);
}
__device__ __forceinline__ u32x4 ldnt(const void* p) { return __builtin_nontemporal_load(reinterpret_cast<const u32x4*>(p)); }

__device__ __forceinline__ void st_gran(u64* g, unsigned v) {
  __hip_atomic_store(g, (1ull << 32) | (u64)v, __ATOMIC_RELAXED, __HIP_MEMORY_SCOPE_AGENT);
}
__device__ __forceinline__ u64 ld_gran(const u64* g) {
  return __hip_atomic_load(g, __ATOMIC_RELAXED, __HIP_MEMORY_SCOPE_AGENT);
}
__device__ __forceinline__ void st_pair(u64* g, float a, float b) {
  const u64 v = ((u64)(__float_as_uint(b) | 1u) << 32) | (u64)(__float_as_uint(a) | 1u);
  __hip_atomic_store(g, v, __ATOMIC_RELAXED, __HIP_MEMORY_SCOPE_AGENT);
}
__device__ __forceinline__ bool pair_ok(u64 v) { return (v & 0x100000001ull) == 0x100000001ull; }
__device__ __forceinline__ float pair_get(u64 v, int comp) { return __uint_as_float((unsigned)(comp ? (v >> 32) : v)); }
constexpr int SPIN_CAP = 1 << 17;

__device__ __forceinline__ unsigned lds_arrive(unsigned* cnt) {
  return __hip_atomic_fetch_add(cnt, 1u, __ATOMIC_ACQ_REL, __HIP_MEMORY_SCOPE_WORKGROUP);
}
__device__ __forceinline__ void lds_set(unsigned* flag) {
  __hip_atomic_store(flag, 1u, __ATOMIC_RELEASE, __HIP_MEMORY_SCOPE_WORKGROUP);
}
__device__ __forceinline__ void lds_wait(unsigned* flag) {
  for (int spins = 0; spins < (1 << 24); ++spins) {
    if (__hip_atomic_load(flag, __ATOMIC_ACQUIRE, __HIP_MEMORY_SCOPE_WORKGROUP) != 0u) break;
    __builtin_amdgcn_s_sleep(1);
  }
}

#define STAMP(i) do {} while (0)

__global__ void __launch_bounds__(NTH, 4) fused_kernel(Params p) {
  __shared__ __attribute__((aligned(16))) float s_xn[DM];
  __shared__ __attribute__((aligned(16))) float s_part[2][8][64];
  __shared__ __attribute__((aligned(16))) unsigned short s_q[256];
  __shared__ __attribute__((aligned(16))) float s_p[8 * 64];
  __shared__ __attribute__((aligned(16))) float s_mrg[8 * 2 * (4 + DH)];
  __shared__ __attribute__((aligned(16))) float s_coef[72];
  __shared__ __attribute__((aligned(16))) float s_g2[8 * DM];
  __shared__ __attribute__((aligned(16))) float s_fin[2][32][16];
  __shared__ __attribute__((aligned(16))) unsigned s_sync[16];

  const int tid = threadIdx.x, lane = tid & 63, wave = __builtin_amdgcn_readfirstlane(tid >> 6);
  const int b = blockIdx.x;
  u64* gr = p.gr;
  if (tid < 16) s_sync[tid] = 0u;
  __syncthreads();
  STAMP(0);

  if (wave < 8) {
    const int g = b >> 4, sp = b & 15;
    const int r = lane >> 4, c = lane & 15;
    u32x4 K[8], V[8];
    lds_wait(&s_sync[6]);
    {
      const size_t key0 = (size_t)g * SK + (SK - WIN) + sp * KPS + wave * 32;
      const bf16* kb = p.kc + key0 * DH + r * DH + c * 8;
      const bf16* vb = p.vc + key0 * DH + r * DH + c * 8;
#pragma unroll
      for (int i = 0; i < 8; ++i) K[i] = ldnt(kb + i * 4 * DH);
#pragma unroll
      for (int i = 0; i < 8; ++i) V[i] = ldnt(vb + i * 4 * DH);
    }
    lds_wait(&s_sync[1]);
    STAMP(3);
    u32x4 q[2];
    q[0] = *reinterpret_cast<const u32x4*>(s_q + c * 8);
    q[1] = *reinterpret_cast<const u32x4*>(s_q + DH + c * 8);
    float x[16];
#pragma unroll
    for (int i = 0; i < 8; ++i)
#pragma unroll
      for (int h = 0; h < 2; ++h) {
        float d = 0.f;
#pragma unroll
        for (int k = 0; k < 4; ++k) d = dot2(K[i][k], q[h][k], d);
        x[i * 2 + h] = d;
      }
#define TR_STEP(HALF, CTRL, BIT) _Pragma("unroll") for (int h = 0; h < (HALF); ++h) { \
      const float lo_ = x[h], hi_ = x[h + (HALF)]; const float t1_ = lo_ + dpp<CTRL>(lo_), t2_ = hi_ + dpp<CTRL>(hi_); \
      x[h] = (c & (BIT)) ? t2_ : t1_; }
    TR_STEP(8, XOR8, 8) TR_STEP(4, XOR7, 4) TR_STEP(2, XOR2, 2) TR_STEP(1, XOR1, 1)
#undef TR_STEP
    const float sc = bf16r(bf16r(x[0]) * 0.08838834764831845f);
    float pm = sc;
    pm = fmaxf(pm, dpp<XOR2>(pm));
    pm = fmaxf(pm, dpp<ROR4>(pm));
    pm = fmaxf(pm, dpp<XOR8>(pm));
    pm = xrow16_max(pm);
    const float pr_ = __expf(sc - pm);
    float l = pr_;
    l += dpp<XOR2>(l); l += dpp<ROR4>(l); l += dpp<XOR8>(l); l = xrow16_sum(l);
    float* prow = s_p + (wave * 4 + r) * 16;
    prow[c] = pr_;
    asm volatile("s_waitcnt lgkmcnt(0)" ::: "memory");
    f32x4 pr[4];
#pragma unroll
    for (int u = 0; u < 4; ++u) pr[u] = *reinterpret_cast<const f32x4*>(prow + 4 * u);
    float acc[16];
#pragma unroll
    for (int k = 0; k < 16; ++k) acc[k] = 0.f;
#pragma unroll
    for (int i = 0; i < 8; ++i) {
      float vf[8];
#pragma unroll
      for (int k = 0; k < 4; ++k) { vf[2 * k] = lo_f(V[i][k]); vf[2 * k + 1] = hi_f(V[i][k]); }
#pragma unroll
      for (int h = 0; h < 2; ++h) {
        const float ph = pr[(i * 2 + h) >> 2][(i * 2 + h) & 3];
#pragma unroll
        for (int k = 0; k < 8; ++k) acc[h * 8 + k] = fmaf(ph, vf[k], acc[h * 8 + k]);
      }
    }
#pragma unroll
    for (int k = 0; k < 16; ++k) acc[k] = xrow16_sum(acc[k]);
    if (r == 0) {
#pragma unroll
      for (int h = 0; h < 2; ++h) {
        float* rec = s_mrg + (wave * 2 + h) * (4 + DH);
        *reinterpret_cast<f32x4*>(rec + 4 + 8 * c) = f32x4{acc[h * 8 + 0], acc[h * 8 + 1], acc[h * 8 + 2], acc[h * 8 + 3]};
        *reinterpret_cast<f32x4*>(rec + 4 + 8 * c + 4) = f32x4{acc[h * 8 + 4], acc[h * 8 + 5], acc[h * 8 + 6], acc[h * 8 + 7]};
        if (c == h) { rec[0] = pm; rec[1] = l; }
      }
    }
    unsigned prev = 0;
    if (lane == 0) prev = lds_arrive(&s_sync[2]);
    prev = __builtin_amdgcn_readfirstlane(prev);
    if (prev == 7u) {
#pragma unroll
      for (int h = 0; h < 2; ++h) {
        float M = -INFINITY;
#pragma unroll
        for (int w = 0; w < 8; ++w) M = fmaxf(M, s_mrg[(w * 2 + h) * (4 + DH)]);
        float L = 0.f, o0 = 0.f, o1 = 0.f;
#pragma unroll
        for (int w = 0; w < 8; ++w) {
          const float* rec = s_mrg + (w * 2 + h) * (4 + DH);
          const float wt = __expf(rec[0] - M);
          L = fmaf(wt, rec[1], L);
          o0 = fmaf(wt, rec[4 + lane], o0);
          o1 = fmaf(wt, rec[4 + 64 + lane], o1);
        }
        u64* pg = gr + G_PART + ((g * 2 + h) * NSPLIT + sp) * 65;
        st_pair(pg + 1 + lane, o0, o1);
        if (lane == 0) st_pair(pg, M, L);
      }
    }
    STAMP(4);
    return;
  }

  const int gw = wave - 8;
  const bool isq = b < 64, isgu = (b >= 64) && (b < 136);
  const int cb = isq ? b : b - 64;
  const int rr = lane >> 3, ch = lane & 7;
  float xv[9];
#pragma unroll
  for (int k = 0; k < 9; ++k) xv[k] = bf2f(reinterpret_cast<const unsigned short*>(p.x)[lane + 64 * k]);
  u32x4 w1a[9], w1b[9];
  {
    const bf16* pa = isq ? p.wq + (size_t)(gw * 72 + rr) * NQ + cb * 64 + ch * 8
                         : (isgu ? p.wg + (size_t)(gw * 72 + rr) * FF + cb * 64 + ch * 8 : p.x + ch * 8);
    const size_t sa = isq ? (size_t)8 * NQ : (isgu ? (size_t)8 * FF : 0);
    const bf16* pb = isgu ? p.wu + (size_t)(gw * 72 + rr) * FF + cb * 64 + ch * 8 : p.x + ch * 8;
    const size_t sb = isgu ? (size_t)8 * FF : 0;
#pragma unroll
    for (int j = 0; j < 9; ++j) w1a[j] = ldnt(pa + j * sa);
#pragma unroll
    for (int j = 0; j < 9; ++j) w1b[j] = ldnt(pb + j * sb);
  }
  {
    float s = 0.f;
#pragma unroll
    for (int k = 0; k < 9; ++k) s += xv[k];
    const float mean = wave_sum(s) * (1.0f / 576.0f);
    float s2 = 0.f;
#pragma unroll
    for (int k = 0; k < 9; ++k) { const float dv = xv[k] - mean; s2 = fmaf(dv, dv, s2); }
    const float var = wave_sum(s2) * (1.0f / 576.0f);
    const float m_bf = bf16r(mean), v_bf = bf16r(var);
    const float den = bf16r(sqrtf(bf16r(v_bf + 1e-5f)));
#pragma unroll
    for (int k = 0; k < 9; ++k) s_xn[lane + 64 * k] = bf16r(bf16r(xv[k] - m_bf) / den);
    asm volatile("s_waitcnt lgkmcnt(0)" ::: "memory");
  }
  if (gw == 0 && lane == 0) lds_set(&s_sync[6]);
  STAMP(1);
  {
    float acc[8], acb[8];
#pragma unroll
    for (int k = 0; k < 8; ++k) { acc[k] = 0.f; acb[k] = 0.f; }
#pragma unroll
    for (int j = 0; j < 9; ++j) {
      const float xr = s_xn[gw * 72 + j * 8 + rr];
#pragma unroll
      for (int k = 0; k < 4; ++k) {
        acc[2 * k] = fmaf(xr, lo_f(w1a[j][k]), acc[2 * k]);
        acc[2 * k + 1] = fmaf(xr, hi_f(w1a[j][k]), acc[2 * k + 1]);
        acb[2 * k] = fmaf(xr, lo_f(w1b[j][k]), acb[2 * k]);
        acb[2 * k + 1] = fmaf(xr, hi_f(w1b[j][k]), acb[2 * k + 1]);
      }
    }
#pragma unroll
    for (int k = 0; k < 8; ++k) {
      float a = acc[k]; a += dpp<XOR8>(a); acc[k] = xrow16_sum(a);
      float bb = acb[k]; bb += dpp<XOR8>(bb); acb[k] = xrow16_sum(bb);
    }
    if (lane < 8) {
#pragma unroll
      for (int k = 0; k < 8; ++k) { s_part[0][gw][ch * 8 + k] = acc[k]; s_part[1][gw][ch * 8 + k] = acb[k]; }
    }
    unsigned prev = 0;
    if (lane == 0) prev = lds_arrive(&s_sync[0]);
    prev = __builtin_amdgcn_readfirstlane(prev);
    if (prev == 7u) {
      if (isq) {
        float a = 0.f;
#pragma unroll
        for (int w = 0; w < 8; ++w) a += s_part[0][w][lane];
        st_gran(gr + G_Q + cb * 64 + lane, __float_as_uint(bf16r(a)) >> 16);
      } else if (isgu) {
        float a = 0.f, bb = 0.f;
#pragma unroll
        for (int w = 0; w < 8; ++w) { a += s_part[0][w][lane]; bb += s_part[1][w][lane]; }
        const float gg = bf16r(a), uu = bf16r(bb);
        const float sg = bf16r(gg / (1.0f + __expf(-gg)));
        st_gran(gr + G_H + cb * 64 + lane, __float_as_uint(bf16r(sg * uu)));
      }
    }
  }
  STAMP(2);
  if (gw == 0) {
    const u64* qg = gr + G_Q + (b >> 4) * 256 + lane;
    unsigned qv[4];
    for (int spins = 0; spins < SPIN_CAP; ++spins) {
      bool ok = true;
#pragma unroll
      for (int k = 0; k < 4; ++k) { const u64 x = ld_gran(qg + k * 64); qv[k] = (unsigned)x; ok &= (x >> 32) == 1ull; }
      if (__all(ok)) break;
      __builtin_amdgcn_s_sleep(1);
    }
#pragma unroll
    for (int k = 0; k < 4; ++k) s_q[lane + k * 64] = (unsigned short)qv[k];
    if (lane == 0) lds_set(&s_sync[1]);
  }
  if (b >= 128) return;

  const int nrows = (b < 64) ? 8 : 9;
  u32x4 w2[9], w2x[2];
  {
    const bf16* slab = (b < 64) ? p.wo + (size_t)b * 64 * DM : p.wd + (size_t)(b - 64) * 72 * DM;
    const bf16* pm = slab + (size_t)gw * DM + lane * 8;
#pragma unroll
    for (int j = 0; j < 9; ++j) { const int jj = j < nrows ? j : 0; w2[j] = ldnt(pm + (size_t)jj * 8 * DM); }
    w2x[0] = ldnt(slab + (size_t)(gw * 8 + (lane >> 3)) * DM + 512 + (lane & 7) * 8);
    w2x[1] = ldnt(slab + (size_t)((b < 64) ? 0 : 64 + gw) * DM + 512 + (lane & 7) * 8);
  }
  if (gw == 0) {
    if (b < 64) {
      const int head = b >> 1, half = b & 1;
      const u64* pg = gr + G_PART + head * NSPLIT * 65;
      unsigned mv = 0, lv = 0, ov[NSPLIT];
      for (int spins = 0; spins < SPIN_CAP; ++spins) {
        const u64 a = ld_gran(pg + (lane & 15) * 65);
        mv = (unsigned)a; lv = (unsigned)(a >> 32);
        bool ok = pair_ok(a);
#pragma unroll
        for (int s = 0; s < NSPLIT; ++s) {
          const u64 cq = ld_gran(pg + s * 65 + 1 + lane);
          ov[s] = __float_as_uint(pair_get(cq, half));
          ok &= pair_ok(cq);
        }
        if (__all(ok)) break;
        __builtin_amdgcn_s_sleep(1);
      }
      const float ms = __uint_as_float(mv);
      float M = ms;
      M = fmaxf(M, dpp<XOR1>(M)); M = fmaxf(M, dpp<XOR2>(M)); M = fmaxf(M, dpp<XOR7>(M)); M = fmaxf(M, dpp<XOR8>(M));
      const float wt = __expf(ms - M);
      float L = wt * __uint_as_float(lv);
      L += dpp<XOR1>(L); L += dpp<XOR2>(L); L += dpp<XOR7>(L); L += dpp<XOR8>(L);
      float o = 0.f;
#pragma unroll
      for (int s = 0; s < NSPLIT; ++s) {
        const float ws_ = __uint_as_float(__builtin_amdgcn_readlane(__float_as_uint(wt), s));
        o = fmaf(ws_, __uint_as_float(ov[s]), o);
      }
      s_coef[lane] = bf16r(o / L);
    } else {
      const u64* hg = gr + G_H + (b - 64) * 72;
      unsigned hv[2];
      for (int spins = 0; spins < SPIN_CAP; ++spins) {
        const u64 a = ld_gran(hg + lane);
        const u64 bq = ld_gran(hg + 64 + (lane & 7));
        hv[0] = (unsigned)a; hv[1] = (unsigned)bq;
        const bool ok = ((a >> 32) == 1ull) & ((bq >> 32) == 1ull);
        if (__all(ok)) break;
        __builtin_amdgcn_s_sleep(1);
      }
      s_coef[lane] = __uint_as_float(hv[0]);
      if (lane < 8) s_coef[64 + lane] = __uint_as_float(hv[1]);
    }
    if (lane == 0) lds_set(&s_sync[3]);
  } else {
    lds_wait(&s_sync[3]);
  }
  STAMP(5);
  {
    float acc[8], acx[8];
#pragma unroll
    for (int k = 0; k < 8; ++k) { acc[k] = 0.f; acx[k] = 0.f; }
#pragma unroll
    for (int j = 0; j < 9; ++j) {
      const float hv = (j < nrows) ? s_coef[j * 8 + gw] : 0.f;
#pragma unroll
      for (int k = 0; k < 4; ++k) {
        acc[2 * k] = fmaf(hv, lo_f(w2[j][k]), acc[2 * k]);
        acc[2 * k + 1] = fmaf(hv, hi_f(w2[j][k]), acc[2 * k + 1]);
      }
    }
    {
      const float h0 = s_coef[gw * 8 + (lane >> 3)];
      const float h1 = (b >= 64 && lane < 8) ? s_coef[64 + gw] : 0.f;
#pragma unroll
      for (int k = 0; k < 4; ++k) {
        acx[2 * k] = fmaf(h1, lo_f(w2x[1][k]), h0 * lo_f(w2x[0][k]));
        acx[2 * k + 1] = fmaf(h1, hi_f(w2x[1][k]), h0 * hi_f(w2x[0][k]));
      }
#pragma unroll
      for (int k = 0; k < 8; ++k) { float a = acx[k]; a += dpp<XOR8>(a); acx[k] = xrow16_sum(a); }
    }
    *reinterpret_cast<f32x4*>(s_g2 + gw * DM + lane * 8) = f32x4{acc[0], acc[1], acc[2], acc[3]};
    *reinterpret_cast<f32x4*>(s_g2 + gw * DM + lane * 8 + 4) = f32x4{acc[4], acc[5], acc[6], acc[7]};
    if (lane < 8) {
      *reinterpret_cast<f32x4*>(s_g2 + gw * DM + 512 + lane * 8) = f32x4{acx[0], acx[1], acx[2], acx[3]};
      *reinterpret_cast<f32x4*>(s_g2 + gw * DM + 512 + lane * 8 + 4) = f32x4{acx[4], acx[5], acx[6], acx[7]};
    }
    unsigned prev = 0;
    if (lane == 0) prev = lds_arrive(&s_sync[4]);
    prev = __builtin_amdgcn_readfirstlane(prev);
    if (prev == 7u) {
      float sv[9];
#pragma unroll
      for (int k = 0; k < 9; ++k) {
        float s = 0.f;
#pragma unroll
        for (int w = 0; w < 8; ++w) s += s_g2[w * DM + lane + 64 * k];
        sv[k] = s;
      }
#pragma unroll
      for (int j = 0; j < 4; ++j) st_pair(gr + G_SP + b * 320 + j * 64 + lane, sv[2 * j], sv[2 * j + 1]);
      st_pair(gr + G_SP + b * 320 + 256 + lane, sv[8], sv[8]);
    }
  }
  STAMP(6);
  if (b >= NRED) return;

  {
    const int t = tid - 512;
    const int col = b * 16 + (t & 15), grp = t >> 4;
    const int gi = col < 512 ? (col >> 7) * 64 + (col & 63) : 256 + (col - 512);
    const int comp = col < 512 ? ((col >> 6) & 1) : 0;
    const u64* sg = gr + G_SP + gi;
    float v[4];
    for (int spins = 0; spins < SPIN_CAP; ++spins) {
      const u64 x0 = ld_gran(sg + (size_t)grp * 320);
      const u64 x1 = ld_gran(sg + (size_t)(grp + 32) * 320);
      const u64 x2 = ld_gran(sg + (size_t)(64 + grp) * 320);
      const u64 x3 = ld_gran(sg + (size_t)(96 + grp) * 320);
      v[0] = pair_get(x0, comp); v[1] = pair_get(x1, comp); v[2] = pair_get(x2, comp); v[3] = pair_get(x3, comp);
      const bool ok = pair_ok(x0) & pair_ok(x1) & pair_ok(x2) & pair_ok(x3);
      if (__all(ok)) break;
      __builtin_amdgcn_s_sleep(1);
    }
    s_fin[0][grp][t & 15] = v[0] + v[1];
    s_fin[1][grp][t & 15] = v[2] + v[3];
    unsigned prev = 0;
    if (lane == 0) prev = lds_arrive(&s_sync[5]);
    prev = __builtin_amdgcn_readfirstlane(prev);
    STAMP(7);
    if (prev == 7u && lane < 16) {
      float st = 0.f, ft = 0.f;
#pragma unroll
      for (int w = 0; w < 32; ++w) { st += s_fin[0][w][lane]; ft += s_fin[1][w][lane]; }
      const float sv = bf16r(st), fv = bf16r(ft);
      const float gv = bf16r(fv / (1.0f + __expf(-fv)));
      const float xo = bf2f(reinterpret_cast<const unsigned short*>(p.x)[b * 16 + lane]);
      const float o = bf16r(xo + bf16r(sv * gv));
      reinterpret_cast<unsigned short*>(p.out)[b * 16 + lane] = (unsigned short)(__float_as_uint(o) >> 16);
    }
  }
}

extern "C" void kernel_launch(void* const* d_in, const int* in_sizes, int n_in,
                              void* d_out, int out_size, void* d_ws, size_t ws_size,
                              hipStream_t stream) {
  Params p;
  p.x = (const bf16*)d_in[0];
  p.kc = (const bf16*)d_in[1];
  p.vc = (const bf16*)d_in[2];
  p.wq = (const bf16*)d_in[3];
  p.wo = (const bf16*)d_in[4];
  p.wg = (const bf16*)d_in[6];
  p.wu = (const bf16*)d_in[7];
  p.wd = (const bf16*)d_in[8];
  p.out = (bf16*)d_out;
  p.gr = (u64*)d_ws;
  (void)hipMemsetAsync(d_ws, 0, GRAN_BYTES, stream);
  fused_kernel<<<NWG, NTH, 0, stream>>>(p);
}
```
